```python
import torch

def _softmax(x, axis=-1):
    return torch.softmax(x, dim=axis)
def _gelu(x):
    return torch.nn.functional.gelu(x, approximate="tanh")
def _silu(x):
    return torch.nn.functional.silu(x)
def _sigmoid(x):
    return torch.sigmoid(x)
def _layernorm(x, axis=-1, eps=1e-5):
    m = x.mean(dim=axis, keepdim=True)
    v = x.var(dim=axis, keepdim=True, unbiased=False)
    return (x - m) / torch.sqrt(v + eps)
def _scatter_add(tgt, idx, src, axis=0):
    return tgt.clone().index_add_(axis, idx, src)
def _segment_sum(src, seg, n):
    out = torch.zeros((n,) + src.shape[1:], dtype=src.dtype)
    return out.index_add_(0, seg, src)
def _pad(x, pw):
    flat = tuple(v for pair in reversed(pw) for v in pair)
    return torch.nn.functional.pad(x, flat)

kernel_name = 'interleave_layers__linear_attn_block__linear_kernel_hedgehog__prompt'

def setup_inputs():

    _w = (lambda a: (lambda t: t.to(torch.bfloat16) if t.is_floating_point() else t)(torch.as_tensor(a)))
    def _A_setup():
        B, S, D = 1, 2048, 640
        return {
            "x": _w((lambda *s: torch.randn(*s, dtype=torch.bfloat16) * 0.4)(B, S, D)),
            "wqkv": _w((lambda *s: torch.randn(*s, dtype=torch.bfloat16) * 0.8)(D, 3 * D)),
            "wo": _w((lambda *s: torch.randn(*s, dtype=torch.bfloat16) * 0.8)(D, D)),
        }
    _A_in = _A_setup()

    def _B_setup():
        B, S, D, H = 1, 2048, 640, 32
        Dh = D // H
        return {
            "x": _w((lambda *s: torch.randn(*s, dtype=torch.bfloat16) * 0.4)(B, S, D)),
            "wqkv": _w((lambda *s: torch.randn(*s, dtype=torch.bfloat16) * 0.8)(D, 3 * D)),
            "w_phi": _w((lambda *s: torch.randn(*s, dtype=torch.bfloat16) * 0.8)(H, Dh, Dh) * (Dh ** -0.5)),
            "wo": _w((lambda *s: torch.randn(*s, dtype=torch.bfloat16) * 0.4)(D, D)),
        }
    _B_in = _B_setup()

    return {
        "A_x": _A_in["x"],
        "A_wqkv": _A_in["wqkv"],
        "A_wo": _A_in["wo"],
        "B_x": _B_in["x"],
        "B_wqkv": _B_in["wqkv"],
        "B_w_phi": _B_in["w_phi"],
        "B_wo": _B_in["wo"],
    }

def reference(A_x, A_wqkv, A_wo, B_x, B_wqkv, B_w_phi, B_wo):
    def _A_ref(x, wqkv, wo):
        B, S, D = x.shape
        H = 32; Dh = D // H
        qkv = (lambda x, *d: x.permute(*d))((x @ wqkv).reshape(B, S, 3, H, Dh), 2, 0, 3, 1, 4)
        q, k, v = qkv[0], qkv[1], qkv[2]
        phi = lambda t: torch.where(t > 0, t + 1, torch.exp(t)) + 1e-6
        pq, pk = phi(q), phi(k)
        kv = torch.einsum("bhsd,bhse->bhde", pk, v)
        z = torch.einsum("bhsd->bhd", pk)
        num = torch.einsum("bhqd,bhde->bhqe", pq, kv)
        den = torch.einsum("bhqd,bhd->bhq", pq, z)[..., None] + 1e-6
        o = num / den
        return (lambda x, *d: x.permute(*d))(o, 0, 2, 1, 3).reshape(B, S, D) @ wo

    def _B_ref(x, wqkv, w_phi, wo):
        B, S, D = x.shape
        H = 32; Dh = D // H
        qkv = (lambda x, *d: x.permute(*d))((x @ wqkv).reshape(B, S, 3, H, Dh), 2, 0, 3, 1, 4)
        q, k, v = qkv[0], qkv[1], qkv[2]
        pq = torch.einsum("bhsd,hdf->bhsf", q, w_phi)
        pk = torch.einsum("bhsd,hdf->bhsf", k, w_phi)
        phi_q = torch.concatenate([_softmax(pq, axis=-1), _softmax(-pq, axis=-1)], axis=-1)
        phi_k = torch.concatenate([_softmax(pk, axis=-1), _softmax(-pk, axis=-1)], axis=-1)
        kv = torch.einsum("bhsf,bhsd->bhfd", phi_k, v)
        z  = torch.einsum("bhsf->bhf", phi_k)
        num = torch.einsum("bhqf,bhfd->bhqd", phi_q, kv)
        den = torch.einsum("bhqf,bhf->bhq", phi_q, z)[..., None] + 1e-6
        o = num / den
        return (lambda x, *d: x.permute(*d))(o, 0, 2, 1, 3).reshape(B, S, D) @ wo

    x = A_x
    x = x + _A_ref(_layernorm(x, axis=-1), A_wqkv, A_wo)
    x = x + _B_ref(_layernorm(x, axis=-1), B_wqkv, B_w_phi, B_wo)
    x = x + _A_ref(_layernorm(x, axis=-1), A_wqkv, A_wo)
    x = x + _B_ref(_layernorm(x, axis=-1), B_wqkv, B_w_phi, B_wo)
    return x
```

```cpp
#include <hip/hip_runtime.h>
#include <hip/hip_bf16.h>
#include <stdint.h>
typedef unsigned short u16;
__device__ __forceinline__ float bf2f(u16 v){ return __uint_as_float(((unsigned)v)<<16); }
__device__ __forceinline__ u16 f2bf(float f){ __hip_bfloat16 b = __float2bfloat16(f); return *(u16*)&b; }
__device__ __forceinline__ float rbf(float f){ return bf2f(f2bf(f)); }
constexpr int S=2048, D=640, H=32, DH=20;

__global__ void k_init(const u16* x, float* xf){ int i=blockIdx.x*256+threadIdx.x; if(i<S*D) xf[i]=bf2f(x[i]); }
__global__ void k_ln(const float* x, float* xn){
  int r=blockIdx.x, t=threadIdx.x; float s=0; for(int i=t;i<D;i+=64) s+=x[r*D+i];
  for(int o=32;o;o>>=1) s+=__shfl_xor(s,o); float m=s/D; float v=0; for(int i=t;i<D;i+=64){float d=x[r*D+i]-m; v+=d*d;}
  for(int o=32;o;o>>=1) v+=__shfl_xor(v,o); v/=D; float rs=1.0f/sqrtf(v+1e-5f);
  for(int i=t;i<D;i+=64) xn[r*D+i]=(x[r*D+i]-m)*rs; }
__global__ void k_gemm(const float* A, const u16* W, float* C, int N, const float* res){
  __shared__ float a[D]; int r=blockIdx.x; for(int i=threadIdx.x;i<D;i+=256) a[i]=A[r*D+i]; __syncthreads();
  for(int n=threadIdx.x;n<N;n+=256){ float acc=0; for(int k=0;k<D;k++) acc+=a[k]*bf2f(W[k*N+n]); if(res) acc=rbf(acc+res[r*N+n]); C[r*N+n]=acc; } }
__global__ void k_phi(const float* qkv, const u16* wphi, float* pq, float* pk, int F){
  int i=blockIdx.x*256+threadIdx.x; if(i>=S*H*2) return; int which=i&1, h=(i>>1)%H, s=(i>>1)/H;
  const float* t=qkv+s*3*D+which*D+h*DH; float* o=(which?pk:pq)+(s*H+h)*40;
  if(F==20){ for(int d=0;d<DH;d++){ float v=t[d]; o[d]=(v>0?v+1:expf(v))+1e-6f; } }
  else { float mx=-1e30f,mn=1e30f; for(int f=0;f<DH;f++){ float acc=0; for(int d=0;d<DH;d++) acc+=t[d]*bf2f(wphi[(h*DH+d)*DH+f]); o[f]=acc; mx=fmaxf(mx,acc); mn=fminf(mn,acc);}
    float sp=0,sn=0; for(int f=0;f<DH;f++){ float p=o[f]; float a=expf(p-mx), b=expf(mn-p); o[f]=a; o[20+f]=b; sp+=a; sn+=b; }
    for(int f=0;f<DH;f++){ o[f]/=sp; o[20+f]/=sn; } } }
__global__ void k_kv(const float* qkv, const float* pk, float* kv, int F){
  int h=blockIdx.x; int i=threadIdx.x; if(i>=F*21) return; int f=i/21, e=i%21; float acc=0;
  for(int s=0;s<S;s++){ float p=pk[(s*H+h)*40+f]; float v= e<20? qkv[s*3*D+2*D+h*DH+e]:1.0f; acc+=p*v; }
  kv[(h*40+f)*21+e]=acc; }
__global__ void k_o(const float* pq, const float* kv, float* o, int F){ int i=blockIdx.x*256+threadIdx.x; if(i>=S*D) return; int e=i%DH, h=(i/DH)%H, s=i/D;
  float num=0,den=0; for(int f=0;f<F;f++){ float p=pq[(s*H+h)*40+f]; num+=p*kv[(h*40+f)*21+e]; den+=p*kv[(h*40+f)*21+20]; }
  o[i]=num/(den+1e-6f); }
__global__ void k_out(const float* xf, u16* out){ int i=blockIdx.x*256+threadIdx.x; if(i<S*D) out[i]=f2bf(xf[i]); }

extern "C" void kernel_launch(void* const* d_in, const int* in_sizes, int n_in, void* d_out, int out_size, void* d_ws, size_t ws_size, hipStream_t stream) {
  const u16* Ax=(const u16*)d_in[0]; const u16* Aw=(const u16*)d_in[1]; const u16* Awo=(const u16*)d_in[2];
  const u16* Bw=(const u16*)d_in[4]; const u16* Bphi=(const u16*)d_in[5]; const u16* Bwo=(const u16*)d_in[6];
  float* ws=(float*)d_ws; float* x=ws; float* x2=x+S*D; float* xn=x2+S*D; float* qkv=xn+S*D; float* pq=qkv+S*3*D; float* pk=pq+S*H*40; float* kv=pk+S*H*40; float* o=kv+H*40*21+64;
  k_init<<<S*D/256,256,0,stream>>>(Ax,x);
  for(int l=0;l<4;l++){ bool B=l&1; int F=B?40:20;
    k_ln<<<S,64,0,stream>>>(x,xn);
    k_gemm<<<S,256,0,stream>>>(xn,B?Bw:Aw,qkv,3*D,nullptr);
    k_phi<<<S*H*2/256,256,0,stream>>>(qkv,Bphi,pq,pk,F);
    k_kv<<<H,1024,0,stream>>>(qkv,pk,kv,F);
    k_o<<<S*D/256,256,0,stream>>>(pq,kv,o,F);
    k_gemm<<<S,256,0,stream>>>(o,B?Bwo:Awo,x2,D,x);
    float* t=x; x=x2; x2=t; }
  k_out<<<S*D/256,256,0,stream>>>(x,(u16*)d_out);
}
```

```cpp
#include <hip/hip_runtime.h>
#include <stdint.h>

#define GAS __attribute__((address_space(1)))
#define LAS __attribute__((address_space(3)))
#define DI __device__ __forceinline__
#define RLX_AGENT __ATOMIC_RELAXED, __HIP_MEMORY_SCOPE_AGENT
typedef unsigned short u16;
typedef GAS unsigned gu32;
typedef unsigned v4u __attribute__((ext_vector_type(4)));
typedef unsigned v2u __attribute__((ext_vector_type(2)));
typedef short s16x4 __attribute__((ext_vector_type(4)));
typedef __bf16 bf16x2 __attribute__((ext_vector_type(2)));
typedef __bf16 bf16x8 __attribute__((ext_vector_type(8)));
typedef float f32x2 __attribute__((ext_vector_type(2)));
typedef float f32x4 __attribute__((ext_vector_type(4)));
typedef float f32x16 __attribute__((ext_vector_type(16)));

#define NLAYERS 4
#define STAMPS 0

constexpr int FL_OFF = 0;
constexpr int FL_BYTES = 3 * 256 * 256;
constexpr int TMO_OFF = FL_BYTES;
constexpr int WT_OFF = FL_BYTES + 256;
constexpr int WT_IMG = 153600;
constexpr int WT_SLICE = WT_IMG + 8192;
constexpr int WOT_OFF = WT_OFF + 2 * 16 * WT_SLICE;
constexpr int XB_OFF = WOT_OFF + 2 * 819200;
constexpr int XB_SZ = 64 * 40 * 1024;
constexpr int OB_OFF = XB_OFF + 3 * XB_SZ;
constexpr int OB_SZ = 64 * 16 * 2560;
constexpr int KVP_OFF = OB_OFF + 4 * OB_SZ;
constexpr int KVP_ONE = 3456;
constexpr int KVP_SZ = 256 * KVP_ONE;
constexpr int ST_OFF = KVP_OFF + 4 * KVP_SZ;
constexpr int WS_BYTES = ST_OFF + 256 * 64 * 8;

constexpr int L_K = 0, L_V = 40960, L_Q = 81920, L_T3 = 122880;
constexpr int L_MISC = 153600;
constexpr int L_WPH = L_MISC;
constexpr int L_ONES = L_MISC + 6144;
constexpr int L_ZERO = L_ONES + 16;
constexpr int L_BYTES = 163840;
constexpr int L_IPK0 = 0, L_IPK1 = 16384, L_IV = 32768;
constexpr int L_KVI = L_Q;
constexpr int KVI_HS = 21 * 96;
constexpr int L_RED = L_Q;
constexpr int L_RED4 = L_Q + 49152;

constexpr int AUX_SC1 = 16;
constexpr float LOG2E = 1.4426950408889634f;

struct Params {
  const u16* x0; const u16* wA; const u16* woA; const u16* wB; const u16* wphi; const u16* woB;
  u16* out; unsigned char* ws;
};

DI unsigned pk2(float a, float b) { f32x2 v = {a, b}; return __builtin_bit_cast(unsigned, __builtin_convertvector(v, bf16x2)); }
DI float rbf(float a) { return __uint_as_float(pk2(a, a) << 16); }
DI float bflo(unsigned d) { return __uint_as_float(d << 16); }
DI float bfhi(unsigned d) { return __uint_as_float(d & 0xffff0000u); }
DI f32x16 mfma(v4u a, v4u b, f32x16 c) {
  return __builtin_amdgcn_mfma_f32_32x32x16_bf16(__builtin_bit_cast(bf16x8, a), __builtin_bit_cast(bf16x8, b), c, 0, 0, 0);
}
DI float dot2(unsigned a, unsigned b, float c) {
  return __builtin_amdgcn_fdot2_f32_bf16(__builtin_bit_cast(bf16x2, a), __builtin_bit_cast(bf16x2, b), c, false);
}
DI v4u lds128(LAS unsigned char* L, int off) { return *(LAS v4u*)(L + off); }
DI void sts128(LAS unsigned char* L, int off, v4u v) { *(LAS v4u*)(L + off) = v; }
DI v4u trfrag(LAS unsigned char* L, int a0, int a1) {
  s16x4 lo = __builtin_amdgcn_ds_read_tr16_b64_v4i16((LAS s16x4*)(L + a0));
  s16x4 hi = __builtin_amdgcn_ds_read_tr16_b64_v4i16((LAS s16x4*)(L + a1));
  v2u l = __builtin_bit_cast(v2u, lo), h = __builtin_bit_cast(v2u, hi);
  return v4u{l.x, l.y, h.x, h.y};
}
#define DRAIN() asm volatile("s_waitcnt vmcnt(0)" ::: "memory")
#define SCHED() __builtin_amdgcn_sched_barrier(0)

DI void block_map(int b, int& mt, int& hp) {
  const int x = b & 7, i = b >> 3;
  mt = 4 * (x >> 1) + (i & 3);
  hp = 8 * (x & 1) + (i >> 2);
}

DI void transpose8x4(const v2u (&in)[8], v4u (&o)[4]) {
#pragma unroll
  for (int d = 0; d < 4; ++d) {
    o[0][d] = __builtin_amdgcn_perm(in[2 * d + 1].x, in[2 * d].x, 0x05040100u);
    o[1][d] = __builtin_amdgcn_perm(in[2 * d + 1].x, in[2 * d].x, 0x07060302u);
    o[2][d] = __builtin_amdgcn_perm(in[2 * d + 1].y, in[2 * d].y, 0x05040100u);
    o[3][d] = __builtin_amdgcn_perm(in[2 * d + 1].y, in[2 * d].y, 0x07060302u);
  }
}

__global__ void __launch_bounds__(256) prep_kernel(Params p) {
  __shared__ float cs[2 * 5 * 30 * 4];
  const int tid = threadIdx.x, b = blockIdx.x;
  const int j = b >> 4, hp = b & 15;
  unsigned char* ws = p.ws;
  if (tid < 48) *(v4u*)(ws + FL_OFF + b * 768 + tid * 16) = v4u{0, 0, 0, 0};
  if (b == 0 && tid == 0) *(unsigned*)(ws + TMO_OFF) = 0;
  for (int t = tid; t < 300; t += 256) {
    const int L = t / 150, rem = t % 150, g5 = rem / 30, ch = rem % 30;
    const int sec = ch / 10, hh = (ch % 10) / 5, cc = ch % 5;
    const u16* W = L ? p.wB : p.wA;
    const int col0 = sec * 640 + (2 * hp + hh) * 20 + 4 * cc, k0 = 40 * j + 8 * g5;
    v2u in[8];
#pragma unroll
    for (int q = 0; q < 8; ++q) in[q] = *(const v2u*)(W + (k0 + q) * 1920 + col0);
    v4u o[4];
    transpose8x4(in, o);
    const int g = 5 * j + g5, ks = g >> 1, kh = g & 1;
    const int tl = sec == 1 ? 0 : (sec == 2 ? 1 : 2);
    const int off = cc < 4 ? tl * 40960 + ks * 1024 + (kh * 32 + 8 * cc + 4 * hh) * 16 : L_T3 + ks * 768 + (kh * 24 + 8 * tl + 4 * hh) * 16;
    unsigned char* dst = ws + WT_OFF + (L * 16 + hp) * WT_SLICE + off;
#pragma unroll
    for (int e = 0; e < 4; ++e) *(v4u*)(dst + e * 16) = o[e];
    float s0 = 0, s1 = 0, s2 = 0, s3 = 0;
#pragma unroll
    for (int q = 0; q < 8; ++q) { s0 += bflo(in[q].x); s1 += bfhi(in[q].x); s2 += bflo(in[q].y); s3 += bfhi(in[q].y); }
    *(f32x4*)(cs + t * 4) = f32x4{s0, s1, s2, s3};
  }
  if (tid < 100) {
    const int T = b * 100 + tid, f = T >> 4, sub = T & 15;
    const int u = f % 10, tl = (f / 10) % 5, w = (f / 50) & 3, nq = (f / 200) & 3, L = f / 800;
    const int q3 = sub & 7, kh = sub >> 3, c = ((q3 >> 2) << 1) | (q3 & 1), h = (q3 >> 1) & 1;
    const int t = tl == 0 ? 4 : ((w + tl) & 3);
    const u16* Wo = L ? p.woB : p.woA;
    const int n0 = 160 * nq + 32 * t + 4 * q3;
    v2u in[8];
#pragma unroll
    for (int q = 0; q < 8; ++q) {
      const int hq = u < 8 ? 4 * w + (u >> 1) : 4 * w + 2 * (u - 8) + (q >> 2);
      const int e = u < 8 ? 8 * (u & 1) + q : 16 + (q & 3);
      in[q] = *(const v2u*)(Wo + ((2 * hq + kh) * 20 + e) * 640 + n0);
    }
    v4u o[4];
    transpose8x4(in, o);
    unsigned char* dst = ws + WOT_OFF + f * 1024 + (kh * 32 + 8 * c + 4 * h) * 16;
#pragma unroll
    for (int e = 0; e < 4; ++e) *(v4u*)(dst + e * 16) = o[e];
  }
  __syncthreads();
  if (tid < 240) {
    const int L = tid / 120, rem = tid % 120, ch = rem >> 2, e = rem & 3;
    float s = 0;
#pragma unroll
    for (int g5 = 0; g5 < 5; ++g5) s += cs[(L * 150 + g5 * 30 + ch) * 4 + e];
    const float hi = rbf(s), lo = s - hi;
    const int sec = ch / 10, hh = (ch % 10) / 5, cc = ch % 5;
    const int tl = sec == 1 ? 0 : (sec == 2 ? 1 : 2);
    const int T = cc < 4 ? tl : 3, i = (cc < 4 ? 8 * cc : 8 * tl) + 4 * hh + e;
    unsigned char* dst = ws + WT_OFF + (L * 16 + hp) * WT_SLICE + WT_IMG + T * 1024 + ((j >> 3) * 32 + i) * 16 + (j & 7) * 2;
    *(u16*)dst = (u16)(pk2(hi, hi) & 0xffff);
    *(u16*)(dst + 4096) = (u16)(pk2(lo, lo) & 0xffff);
  }
}

DI void dma2k(const unsigned char* gsrc, unsigned ldsoff, unsigned lane16) {
  unsigned keep;
  asm volatile(
      "s_mov_b32 %0, m0\n\ts_mov_b32 m0, %2\n\ts_nop 3\n\t"
      "global_load_lds_dwordx4 %1, %3\n\tglobal_load_lds_dwordx4 %1, %3 offset:1024\n\t"
      "s_mov_b32 m0, %0"
      : "=&s"(keep) : "v"(lane16), "s"(ldsoff), "s"(gsrc) : "memory");
}
DI void dma_range(const unsigned char* gimg, unsigned ldsbase, int g0, int g1, int first, int step, unsigned lane16) {
  for (int g = g0 + first; g < g1; g += step) dma2k(gimg + g * 2048, ldsbase + g * 2048, lane16);
}

DI void publish(unsigned char* ws, int type, int mt, int hp, unsigned val, int tid) {
  if (tid < 16) {
    const int cu = type == 1 ? tid * 16 + hp : mt * 16 + tid;
    const int slot = type == 1 ? mt : hp;
    __hip_atomic_store((gu32*)(ws + FL_OFF + (type * 256 + cu) * 256 + slot * 4), val, RLX_AGENT);
  }
}
DI void wait_flags(unsigned char* ws, int type, int mt, int hp, unsigned val, int w, int lane) {
  if (w == 0) {
    gu32* f = (gu32*)(ws + FL_OFF + (type * 256 + mt * 16 + hp) * 256 + (lane & 15) * 4);
    gu32* tmo = (gu32*)(ws + TMO_OFF);
    for (unsigned sp = 0;;) {
      const unsigned v = __hip_atomic_load(f, RLX_AGENT);
      if (__all(v >= val)) break;
      if ((++sp & 1023u) == 0u) {
        if (sp > (1u << 18)) { __hip_atomic_store(tmo, 1u, RLX_AGENT); break; }
        if (__hip_atomic_load(tmo, RLX_AGENT) != 0u) break;
      }
    }
    __builtin_amdgcn_fence(__ATOMIC_ACQUIRE, "wavefront");
  }
  __syncthreads();
}

#define STAMP(i) do {} while (0)

DI float phiA(float a, float rs, float rsl) {
  const float e = __builtin_amdgcn_exp2f(fminf(a, 0.f) * rsl) + 1e-6f;
  return fmaf(fmaxf(a, 0.f), rs, e);
}

DI void phiB(LAS unsigned char* L, int lane, const float (&t)[20], unsigned (&pos)[10], unsigned (&neg)[10]) {
  v4u b0 = {pk2(t[0], t[1]), pk2(t[2], t[3]), pk2(t[4], t[5]), pk2(t[6], t[7])};
  v4u b1 = {pk2(t[8], t[9]), pk2(t[10], t[11]), pk2(t[12], t[13]), pk2(t[14], t[15])};
  v4u b2 = {pk2(t[16], t[17]), pk2(t[18], t[19]), 0u, 0u};
  f32x16 d0 = {}, d1 = {};
  const int a = L_WPH + lane * 16;
  d0 = mfma(lds128(L, a), b0, d0);
  d1 = mfma(lds128(L, a + 3072), b0, d1);
  d0 = mfma(lds128(L, a + 1024), b1, d0);
  d1 = mfma(lds128(L, a + 4096), b1, d1);
  d0 = mfma(lds128(L, a + 2048), b2, d0);
  d1 = mfma(lds128(L, a + 5120), b2, d1);
  float pv[20];
#pragma unroll
  for (int i = 0; i < 16; ++i) pv[i] = d0[i];
#pragma unroll
  for (int i = 0; i < 4; ++i) pv[16 + i] = d1[i];
  float mx = pv[0], mn = pv[0];
#pragma unroll
  for (int i = 1; i < 20; ++i) { mx = fmaxf(mx, pv[i]); mn = fminf(mn, pv[i]); }
  const float mxl = -mx * LOG2E, mnl = mn * LOG2E;
  float ea[20], eb[20], sa = 0.f, sb = 0.f;
#pragma unroll
  for (int i = 0; i < 20; ++i) {
    ea[i] = __builtin_amdgcn_exp2f(fmaf(pv[i], LOG2E, mxl));
    eb[i] = __builtin_amdgcn_exp2f(fmaf(pv[i], -LOG2E, mnl));
    sa += ea[i]; sb += eb[i];
  }
  const float ra = __builtin_amdgcn_rcpf(sa), rb = __builtin_amdgcn_rcpf(sb);
#pragma unroll
  for (int i = 0; i < 10; ++i) {
    pos[i] = pk2(ea[2 * i] * ra, ea[2 * i + 1] * ra);
    neg[i] = pk2(eb[2 * i] * rb, eb[2 * i + 1] * rb);
  }
}

__global__ void __launch_bounds__(256, 1) main_kernel(Params p) {
  extern __shared__ __attribute__((aligned(16))) unsigned char smem[];
  LAS unsigned char* L = (LAS unsigned char*)smem;
  const int tid = threadIdx.x, lane = tid & 63, w = __builtin_amdgcn_readfirstlane(tid >> 6);
  const int h = lane >> 5, r = lane & 31;
  int mt, hp;
  block_map(blockIdx.x, mt, hp);
  unsigned char* ws = p.ws;
  const __amdgpu_buffer_rsrc_t rsW = __builtin_amdgcn_make_buffer_rsrc((void*)ws, 0, WS_BYTES, 0x00020000);
  const __amdgpu_buffer_rsrc_t rsX = __builtin_amdgcn_make_buffer_rsrc((void*)p.x0, 0, 2048 * 1280, 0x00020000);
  const __amdgpu_buffer_rsrc_t rsO = __builtin_amdgcn_make_buffer_rsrc((void*)p.out, 0, 2048 * 1280, 0x00020000);
  const unsigned ldsbase = (unsigned)(size_t)L;
  const unsigned lane16 = lane * 16;
  const int tt = 4 * mt + w;
  const int tt2 = 4 * mt + (hp >> 2), nq = hp & 3;
  STAMP(0);

  dma_range(ws + WT_OFF + hp * WT_SLICE, ldsbase, 0, 75, w, 4, lane16);

  float xr[16], xr4[8];
  {
    const int vo = (32 * tt2 + r) * 1280 + h * 16 + nq * 320;
    const v4u a = __builtin_amdgcn_raw_buffer_load_b128(rsX, vo + w * 64, 0, 0);
    const v4u b = __builtin_amdgcn_raw_buffer_load_b128(rsX, vo + w * 64 + 32, 0, 0);
    const v4u c = __builtin_amdgcn_raw_buffer_load_b128(rsX, vo + 256 + (w & 1) * 32, 0, 0);
#pragma unroll
    for (int i = 0; i < 4; ++i) {
      xr[2 * i] = bflo(a[i]); xr[2 * i + 1] = bfhi(a[i]);
      xr[8 + 2 * i] = bflo(b[i]); xr[8 + 2 * i + 1] = bfhi(b[i]);
      xr4[2 * i] = bflo(c[i]); xr4[2 * i + 1] = bfhi(c[i]);
    }
  }
  {
    const int c = r >> 3, hi = (r >> 2) & 1, e = r & 3;
    for (int fr = w; fr < 6; fr += 4) {
      const int tile = fr / 3, s = fr % 3;
      const int f = tile == 0 ? 4 * c + e : 16 + e;
      const bool rowok = (hi == h) && (tile == 0 || c == 0);
      unsigned v[8];
#pragma unroll
      for (int j = 0; j < 8; ++j) {
        const int d = 8 * s + j;
        v[j] = (rowok && d < 20) ? (unsigned)p.wphi[((2 * hp + h) * 20 + d) * 20 + f] : 0u;
      }
      sts128(L, L_WPH + fr * 1024 + lane * 16, v4u{v[0] | (v[1] << 16), v[2] | (v[3] << 16), v[4] | (v[5] << 16), v[6] | (v[7] << 16)});
    }
    if (tid < 6) sts128(L, L_ONES + tid * 16, tid == 0 ? v4u{0x3f803f80u, 0x3f803f80u, 0x3f803f80u, 0x3f803f80u} : v4u{0u, 0u, 0u, 0u});
  }

  for (int l = 0; l < NLAYERS; ++l) {
    const bool isB = l & 1;
    const int wt = WT_OFF + ((l & 1) * 16 + hp) * WT_SLICE;
    DRAIN();
    if (l > 0) wait_flags(ws, 0, mt, hp, l, w, lane);
    else __syncthreads();
    STAMP(1 + l * 12 + 0);
    v4u xf[40], csf[8], wo[50];
    {
      const __amdgpu_buffer_rsrc_t rs = l == 0 ? rsX : rsW;
      const int vo = l == 0 ? (32 * tt + r) * 1280 + h * 16 : XB_OFF + (l - 1) * XB_SZ + tt * 40960 + lane * 16;
      const int step = l == 0 ? 32 : 1024;
#pragma unroll
      for (int ks = 0; ks < 40; ++ks) xf[ks] = __builtin_amdgcn_raw_buffer_load_b128(rs, vo, ks * step, AUX_SC1);
#pragma unroll
      for (int i = 0; i < 8; ++i) csf[i] = __builtin_amdgcn_raw_buffer_load_b128(rsW, lane * 16 + i * 1024, wt + WT_IMG, 0);
      SCHED();
    }
    const int wov = WOT_OFF + (((l & 1) * 4 + nq) * 4 + w) * 51200;
    f32x16 aK = {}, aV = {}, aQ = {}, aT = {};
    float ssq = 0.f;
    {
      int t3a = r < 24 ? L_T3 + (h * 24 + r) * 16 : L_ONES;
      const int t3s = r < 24 ? 768 : 0;
#pragma unroll
      for (int ks = 0; ks < 40; ++ks) {
        const v4u fk = lds128(L, L_K + ks * 1024 + lane * 16);
        const v4u fv = lds128(L, L_V + ks * 1024 + lane * 16);
        const v4u ft = lds128(L, t3a);
        const v4u fq = lds128(L, L_Q + ks * 1024 + lane * 16);
        t3a += t3s;
        const v4u x = xf[ks];
        aK = mfma(fk, x, aK);
        aV = mfma(fv, x, aV);
        aT = mfma(ft, x, aT);
        aQ = mfma(fq, x, aQ);
        ssq = dot2(x.x, x.x, ssq); ssq = dot2(x.y, x.y, ssq); ssq = dot2(x.z, x.z, ssq); ssq = dot2(x.w, x.w, ssq);
        if (ks >= 20) wo[ks - 20] = __builtin_amdgcn_raw_buffer_load_b128(rsW, lane * 16, wov + (ks - 20) * 1024, 0);
      }
    }
    STAMP(1 + l * 12 + 1);
    float rs;
    {
      ssq += __shfl_xor(ssq, 32);
      const float mean = aT[12] * (1.f / 640.f);
      const float var = fmaxf(ssq * (1.f / 640.f) - mean * mean, 0.f);
      const float sd = rbf(sqrtf(rbf(rbf(var) + 1e-5f)));
      rs = 1.0f / sd;
      const unsigned nm = pk2(-mean, -mean);
      const v4u mb = {nm, nm, nm, nm};
      aK = mfma(csf[0], mb, aK); aV = mfma(csf[1], mb, aV); aT = mfma(csf[3], mb, aT); aQ = mfma(csf[2], mb, aQ);
      aK = mfma(csf[4], mb, aK); aV = mfma(csf[5], mb, aV); aT = mfma(csf[7], mb, aT); aQ = mfma(csf[6], mb, aQ);
    }
    const float rsl = rs * LOG2E;
    {
      unsigned pos[10], neg[10], vv[10];
      if (!isB) {
#pragma unroll
        for (int i = 0; i < 8; ++i) pos[i] = pk2(phiA(aK[2 * i], rs, rsl), phiA(aK[2 * i + 1], rs, rsl));
        pos[8] = pk2(phiA(aT[0], rs, rsl), phiA(aT[1], rs, rsl));
        pos[9] = pk2(phiA(aT[2], rs, rsl), phiA(aT[3], rs, rsl));
#pragma unroll
        for (int i = 0; i < 10; ++i) neg[i] = 0u;
      } else {
        float t[20];
#pragma unroll
        for (int i = 0; i < 16; ++i) t[i] = aK[i] * rs;
#pragma unroll
        for (int i = 0; i < 4; ++i) t[16 + i] = aT[i] * rs;
        phiB(L, lane, t, pos, neg);
      }
#pragma unroll
      for (int i = 0; i < 8; ++i) vv[i] = pk2(aV[2 * i] * rs, aV[2 * i + 1] * rs);
      vv[8] = pk2(aT[4] * rs, aT[5] * rs);
      vv[9] = pk2(aT[6] * rs, aT[7] * rs);
      __syncthreads();
      const int row = h * 8192 + (32 * w + r) * 64, sw = (r >> 1) & 3;
      const int u0 = row + (0 ^ sw) * 16, u1 = row + (1 ^ sw) * 16, u2 = row + (2 ^ sw) * 16;
      sts128(L, L_IPK0 + u0, v4u{pos[0], pos[1], pos[2], pos[3]});
      sts128(L, L_IPK0 + u1, v4u{pos[4], pos[5], pos[6], pos[7]});
      sts128(L, L_IPK0 + u2, v4u{pos[8], pos[9], 0u, 0u});
      sts128(L, L_IV + u0, v4u{vv[0], vv[1], vv[2], vv[3]});
      sts128(L, L_IV + u1, v4u{vv[4], vv[5], vv[6], vv[7]});
      sts128(L, L_IV + u2, v4u{vv[8], vv[9], 0x00003f80u, 0u});
      if (isB) {
        sts128(L, L_IPK1 + u0, v4u{neg[0], neg[1], neg[2], neg[3]});
        sts128(L, L_IPK1 + u1, v4u{neg[4], neg[5], neg[6], neg[7]});
        sts128(L, L_IPK1 + u2, v4u{neg[8], neg[9], 0u, 0u});
      }
      __syncthreads();
    }
    STAMP(1 + l * 12 + 2);
    const int nroles = isB ? 4 : 2;
    const int kvp = KVP_OFF + l * KVP_SZ + hp * 16 * KVP_ONE;
    if (w < nroles) {
      const int head = w & 1, part = w >> 1;
      const int g = lane >> 4, kh = g >> 1, blk = g & 1, q = (lane & 15) >> 2, pp = lane & 3;
      const int un = 2 * blk + (pp >> 1);
      const int a0 = head * 8192 + (8 * kh + q) * 64 + ((un ^ (q >> 1)) * 16) + 8 * (pp & 1);
      const int a1 = head * 8192 + (8 * kh + 4 + q) * 64 + ((un ^ (2 + (q >> 1))) * 16) + 8 * (pp & 1);
      const int pb = part * 16384;
      f32x16 d = {};
#pragma unroll
      for (int s = 0; s < 8; ++s) {
        const v4u fa = trfrag(L, pb + a0 + s * 1024, pb + a1 + s * 1024);
        const v4u fb = trfrag(L, L_IV + a0 + s * 1024, L_IV + a1 + s * 1024);
        d = mfma(fa, fb, d);
      }
      if (r < 21) {
        const v4u ua = {pk2(d[0], d[1]), pk2(d[2], d[3]), pk2(d[4], d[5]), pk2(d[6], d[7])};
        __builtin_amdgcn_raw_buffer_store_b128(ua, rsW, ((w * 2 + h) * 21 + r) * 16, kvp + mt * KVP_ONE, AUX_SC1);
        if (h == 0) {
          const v2u ub = {pk2(d[8], d[9]), pk2(d[10], d[11])};
          __builtin_amdgcn_raw_buffer_store_b64(ub, rsW, nroles * 672 + (w * 21 + r) * 8, kvp + mt * KVP_ONE, AUX_SC1);
        }
      }
    }
    DRAIN();
    __syncthreads();
    publish(ws, 1, mt, hp, l + 1, tid);
    STAMP(1 + l * 12 + 3);
#pragma unroll
    for (int i = 20; i < 50; ++i) wo[i] = __builtin_amdgcn_raw_buffer_load_b128(rsW, lane * 16, wov + i * 1024, 0);
    SCHED();
    unsigned pq[20];
    if (!isB) {
#pragma unroll
      for (int i = 0; i < 8; ++i) pq[i] = pk2(phiA(aQ[2 * i], rs, rsl), phiA(aQ[2 * i + 1], rs, rsl));
      pq[8] = pk2(phiA(aT[8], rs, rsl), phiA(aT[9], rs, rsl));
      pq[9] = pk2(phiA(aT[10], rs, rsl), phiA(aT[11], rs, rsl));
#pragma unroll
      for (int i = 10; i < 20; ++i) pq[i] = 0u;
    } else {
      float t[20];
#pragma unroll
      for (int i = 0; i < 16; ++i) t[i] = aQ[i] * rs;
#pragma unroll
      for (int i = 0; i < 4; ++i) t[16 + i] = aT[8 + i] * rs;
      unsigned pos[10], neg[10];
      phiB(L, lane, t, pos, neg);
#pragma unroll
      for (int i = 0; i < 10; ++i) { pq[i] = pos[i]; pq[10 + i] = neg[i]; }
    }
    const int nA = 42 * nroles, nU = nA + (21 * nroles + 1) / 2;
    int d0, d1;
    if (tid < nA) {
      const int role = tid / 42, rem = tid % 42, hh = rem / 21, e = rem % 21;
      d0 = L_KVI + (role & 1) * KVI_HS + e * 96 + (role >> 1) * 40 + hh * 8;
      d1 = d0 + 16;
    } else {
      const int c0 = 2 * (tid - nA), c1 = c0 + 1;
      const int r0 = c0 / 21, e0 = c0 % 21, r1 = c1 / 21, e1 = c1 % 21;
      d0 = L_KVI + (r0 & 1) * KVI_HS + e0 * 96 + (r0 >> 1) * 40 + 32;
      d1 = L_KVI + (r1 & 1) * KVI_HS + e1 * 96 + (r1 >> 1) * 40 + 32;
    }
    STAMP(1 + l * 12 + 4);
    wait_flags(ws, 1, mt, hp, l + 1, w, lane);
    STAMP(1 + l * 12 + 5);
    if (tid < nU) {
      v4u pp[16];
#pragma unroll
      for (int m = 0; m < 16; ++m) pp[m] = __builtin_amdgcn_raw_buffer_load_b128(rsW, tid * 16, kvp + m * KVP_ONE, AUX_SC1);
      SCHED();
      float s[8] = {};
#pragma unroll
      for (int m = 0; m < 16; ++m) {
#pragma unroll
        for (int i = 0; i < 4; ++i) { s[2 * i] += bflo(pp[m][i]); s[2 * i + 1] += bfhi(pp[m][i]); }
      }
      *(LAS v2u*)(L + d0) = v2u{pk2(s[0], s[1]), pk2(s[2], s[3])};
      *(LAS v2u*)(L + d1) = v2u{pk2(s[4], s[5]), pk2(s[6], s[7])};
    }
    __syncthreads();
    STAMP(1 + l * 12 + 6);
    {
      const int c = r >> 3, hi = (r >> 2) & 1, e = r & 3;
      const int b0 = hi == h ? L_KVI + h * KVI_HS + (4 * c + e) * 96 : L_ZERO;
      const int b1 = hi == h ? L_KVI + h * KVI_HS + (c == 0 ? 16 + e : 20) * 96 : L_ZERO;
      f32x16 n0 = {}, n1 = {};
#pragma unroll
      for (int s = 0; s < 2; ++s) {
        const v4u b = {pq[4 * s], pq[4 * s + 1], pq[4 * s + 2], pq[4 * s + 3]};
        n0 = mfma(lds128(L, b0 + 16 * s), b, n0);
        n1 = mfma(lds128(L, b1 + 16 * s), b, n1);
      }
      {
        const v4u b = {pq[8], pq[9], pq[10], pq[11]};
        v4u fa = lds128(L, b0 + 32), fb = lds128(L, b1 + 32);
        if (!isB) { fa.z = 0u; fa.w = 0u; fb.z = 0u; fb.w = 0u; }
        n0 = mfma(fa, b, n0);
        n1 = mfma(fb, b, n1);
      }
      if (isB) {
#pragma unroll
        for (int s = 3; s < 5; ++s) {
          const v4u b = {pq[4 * s], pq[4 * s + 1], pq[4 * s + 2], pq[4 * s + 3]};
          n0 = mfma(lds128(L, b0 + 16 * s), b, n0);
          n1 = mfma(lds128(L, b1 + 16 * s), b, n1);
        }
      }
      const float inv = 1.0f / (n1[4] + 1e-6f);
      const v4u o0 = {pk2(n0[0] * inv, n0[1] * inv), pk2(n0[2] * inv, n0[3] * inv), pk2(n0[4] * inv, n0[5] * inv), pk2(n0[6] * inv, n0[7] * inv)};
      const v4u o1 = {pk2(n0[8] * inv, n0[9] * inv), pk2(n0[10] * inv, n0[11] * inv), pk2(n0[12] * inv, n0[13] * inv), pk2(n0[14] * inv, n0[15] * inv)};
      const v2u o2 = {pk2(n1[0] * inv, n1[1] * inv), pk2(n1[2] * inv, n1[3] * inv)};
      const int ob = OB_OFF + l * OB_SZ + (tt * 16 + hp) * 2560;
      __builtin_amdgcn_raw_buffer_store_b128(o0, rsW, lane * 16, ob, AUX_SC1);
      __builtin_amdgcn_raw_buffer_store_b128(o1, rsW, lane * 16 + 1024, ob, AUX_SC1);
      __builtin_amdgcn_raw_buffer_store_b64(o2, rsW, lane * 8 + 2048, ob, AUX_SC1);
    }
    DRAIN();
    __syncthreads();
    publish(ws, 2, mt, hp, l + 1, tid);
    STAMP(1 + l * 12 + 7);
    const unsigned char* wnext = ws + WT_OFF + (((l + 1) & 1) * 16 + hp) * WT_SLICE;
    if (l + 1 < NLAYERS && w > 0) dma_range(wnext, ldsbase, 0, 40, w - 1, 3, lane16);
    wait_flags(ws, 2, mt, hp, l + 1, w, lane);
    STAMP(1 + l * 12 + 8);
    {
      v4u of[10];
      const int ob = OB_OFF + l * OB_SZ + (tt2 * 16 + 4 * w) * 2560;
      v2u oh[4];
#pragma unroll
      for (int a = 0; a < 4; ++a) {
        of[2 * a] = __builtin_amdgcn_raw_buffer_load_b128(rsW, lane * 16, ob + a * 2560, AUX_SC1);
        of[2 * a + 1] = __builtin_amdgcn_raw_buffer_load_b128(rsW, lane * 16 + 1024, ob + a * 2560, AUX_SC1);
        oh[a] = __builtin_amdgcn_raw_buffer_load_b64(rsW, lane * 8 + 2048, ob + a * 2560, AUX_SC1);
      }
      SCHED();
      of[8] = v4u{oh[0].x, oh[0].y, oh[1].x, oh[1].y};
      of[9] = v4u{oh[2].x, oh[2].y, oh[3].x, oh[3].y};
      f32x16 own = {};
#pragma unroll
      for (int tl = 0; tl < 5; ++tl) {
        f32x16 acc = {};
#pragma unroll
        for (int u = 0; u < 10; ++u) acc = mfma(wo[tl * 10 + u], of[u], acc);
        if (tl < 4) {
          const int dst = (tl == 0 ? L_RED4 + w * 4096 : L_RED + (((w + tl) & 3) * 3 + (3 - tl)) * 4096) + lane * 16;
#pragma unroll
          for (int qd = 0; qd < 4; ++qd)
            sts128(L, dst + qd * 1024, __builtin_bit_cast(v4u, f32x4{acc[4 * qd], acc[4 * qd + 1], acc[4 * qd + 2], acc[4 * qd + 3]}));
        } else own = acc;
      }
      __syncthreads();
      STAMP(1 + l * 12 + 9);
#pragma unroll
      for (int s = 0; s < 3; ++s)
#pragma unroll
        for (int qd = 0; qd < 4; ++qd) {
          const f32x4 v = __builtin_bit_cast(f32x4, lds128(L, L_RED + ((w * 3 + s) * 4 + qd) * 1024 + lane * 16));
#pragma unroll
          for (int i = 0; i < 4; ++i) own[4 * qd + i] += v[i];
        }
#pragma unroll
      for (int i = 0; i < 16; ++i) xr[i] += own[i];
      const bool last = l == NLAYERS - 1;
      const __amdgpu_buffer_rsrc_t rs2 = last ? rsO : rsW;
      const int vo = last ? (32 * tt2 + r) * 1280 + h * 16 + nq * 320 : XB_OFF + l * XB_SZ + tt2 * 40960 + nq * 10240 + lane * 16;
      const int step = last ? 32 : 1024;
      __builtin_amdgcn_raw_buffer_store_b128(v4u{pk2(xr[0], xr[1]), pk2(xr[2], xr[3]), pk2(xr[4], xr[5]), pk2(xr[6], xr[7])}, rs2, vo, 2 * w * step, AUX_SC1);
      __builtin_amdgcn_raw_buffer_store_b128(v4u{pk2(xr[8], xr[9]), pk2(xr[10], xr[11]), pk2(xr[12], xr[13]), pk2(xr[14], xr[15])}, rs2, vo, (2 * w + 1) * step, AUX_SC1);
      if (w < 2) {
        float t4[8] = {};
#pragma unroll
        for (int s = 0; s < 4; ++s)
#pragma unroll
          for (int qd = 0; qd < 2; ++qd) {
            const f32x4 v = __builtin_bit_cast(f32x4, lds128(L, L_RED4 + (s * 4 + 2 * w + qd) * 1024 + lane * 16));
#pragma unroll
            for (int i = 0; i < 4; ++i) t4[4 * qd + i] += v[i];
          }
#pragma unroll
        for (int i = 0; i < 8; ++i) xr4[i] += t4[i];
        __builtin_amdgcn_raw_buffer_store_b128(v4u{pk2(xr4[0], xr4[1]), pk2(xr4[2], xr4[3]), pk2(xr4[4], xr4[5]), pk2(xr4[6], xr4[7])}, rs2, vo, (8 + w) * step, AUX_SC1);
      }
    }
    if (l + 1 < NLAYERS) {
      DRAIN();
      __syncthreads();
      publish(ws, 0, mt, hp, l + 1, tid);
      STAMP(1 + l * 12 + 10);
      if (w > 0) dma_range(wnext, ldsbase, 40, 75, w - 1, 3, lane16);
    }
  }
  STAMP(60);
}

extern "C" void kernel_launch(void* const* d_in, const int* in_sizes, int n_in, void* d_out, int out_size, void* d_ws, size_t ws_size,
                              hipStream_t stream) {
  Params p;
  p.x0 = (const u16*)d_in[0]; p.wA = (const u16*)d_in[1]; p.woA = (const u16*)d_in[2];
  p.wB = (const u16*)d_in[4]; p.wphi = (const u16*)d_in[5]; p.woB = (const u16*)d_in[6];
  p.out = (u16*)d_out; p.ws = (unsigned char*)d_ws;
  (void)hipFuncSetAttribute((const void*)main_kernel, hipFuncAttributeMaxDynamicSharedMemorySize, L_BYTES);
  prep_kernel<<<256, 256, 0, stream>>>(p);
  main_kernel<<<256, 256, L_BYTES, stream>>>(p);
}
```

```cpp
#include <hip/hip_runtime.h>
#include <stdint.h>

#define GAS __attribute__((address_space(1)))
#define LAS __attribute__((address_space(3)))
#define DI __device__ __forceinline__
#define RLX_AGENT __ATOMIC_RELAXED, __HIP_MEMORY_SCOPE_AGENT
typedef unsigned short u16;
typedef unsigned long long u64;
typedef GAS unsigned gu32;
typedef GAS u64 gu64;
typedef unsigned v4u __attribute__((ext_vector_type(4)));
typedef unsigned v2u __attribute__((ext_vector_type(2)));
typedef short s16x4 __attribute__((ext_vector_type(4)));
typedef __bf16 bf16x2 __attribute__((ext_vector_type(2)));
typedef float f32x2 __attribute__((ext_vector_type(2)));
typedef float f32x4 __attribute__((ext_vector_type(4)));
typedef float f32x16 __attribute__((ext_vector_type(16)));
typedef __amdgpu_buffer_rsrc_t rsrc_t;

#define NLAYERS 4
#define STAMPS 0

constexpr int EP_OFF = 0;
constexpr int TG_OFF = 256;
constexpr int CT_OFF = TG_OFF + 256 * 256;
constexpr int WT_OFF = CT_OFF + 3 * 256 * 256;
constexpr int WT_IMG = 153600;
constexpr int CS_OFF = WT_OFF + 32 * WT_IMG;
constexpr int WOA_OFF = CS_OFF + 32 * 8192;
constexpr int WOB_OFF = WOA_OFF + 16 * 204800;
constexpr int XB_OFF = WOB_OFF + 4 * 204800;
constexpr int XB_SZ = 64 * 40 * 1024;
constexpr int SB_OFF = XB_OFF + 4 * XB_SZ;
constexpr int SB_SZ = 64 * 16 * 256;
constexpr int OB_OFF = SB_OFF + 4 * SB_SZ;
constexpr int OB_SZ = 64 * 16 * 2560;
constexpr int KVP_OFF = OB_OFF + 4 * OB_SZ;
constexpr int KVP_ONE = 3456;
constexpr int KVP_SZ = 256 * KVP_ONE;
constexpr int ST_OFF = KVP_OFF + 4 * KVP_SZ;
constexpr int WS_BYTES = ST_OFF + 256 * 64 * 8;

constexpr int L_K = 0, L_V = 40960, L_Q = 81920, L_T3 = 122880;
constexpr int L_WPH = 153600;
constexpr int L_CS = L_WPH + 6144;
constexpr int L_ZERO = L_CS + 2048;
constexpr int L_BYTES = 163840;
constexpr int L_IPK0 = 0, L_IPK1 = 16384, L_IV = 32768;
constexpr int L_KVI = L_Q;
constexpr int KVI_HS = 21 * 96;
constexpr int L_RED = L_Q;
constexpr int L_RED4 = L_Q + 49152;

constexpr int AUX_SC1 = 16;
constexpr float LOG2E = 1.4426950408889634f;
constexpr unsigned SPIN_CAP = 1u << 17;

struct Params {
  const u16* x0; const u16* wA; const u16* woA; const u16* wB; const u16* wphi; const u16* woB;
  u16* out; unsigned char* ws;
};

DI f32x2 mk2(float a, float b) { return f32x2{a, b}; }
DI unsigned pkv(f32x2 v) { return __builtin_bit_cast(unsigned, __builtin_convertvector(v, bf16x2)); }
DI unsigned pk2(float a, float b) { return pkv(mk2(a, b)); }
DI float rbf(float a) { return __uint_as_float(pk2(a, a) << 16); }
DI float bflo(unsigned d) { return __uint_as_float(d << 16); }
DI float bfhi(unsigned d) { return __uint_as_float(d & 0xffff0000u); }
DI float exp2c(float x) { return __builtin_amdgcn_fmed3f(__builtin_amdgcn_exp2f(x), 0.f, 1.f); }

#define MF "v_mfma_f32_32x32x16_bf16 "
#define MFMA0(acc, a, b) asm volatile(MF "%0, %1, %2, 0" : "=&v"(acc) : "v"(a), "v"(b))
#define MFMA(acc, a, b) asm volatile(MF "%0, %1, %2, %0" : "+v"(acc) : "v"(a), "v"(b))
#define MFMA0N(acc, a, b) asm volatile("s_nop 1\n\t" MF "%0, %1, %2, 0" : "=&v"(acc) : "v"(a), "v"(b))
#define MFMAN(acc, a, b) asm volatile("s_nop 1\n\t" MF "%0, %1, %2, %0" : "+v"(acc) : "v"(a), "v"(b))
#define MFMA0A(acc, a, b) asm volatile(MF "%0, %1, %2, 0" : "=&v"(acc) : "a"(a), "v"(b))
#define MFMAA(acc, a, b) asm volatile(MF "%0, %1, %2, %0" : "+v"(acc) : "a"(a), "v"(b))
#define MFMA0B(acc, a, b) asm volatile(MF "%0, %1, %2, 0" : "=&v"(acc) : "v"(a), "a"(b))
#define MFMAB(acc, a, b) asm volatile(MF "%0, %1, %2, %0" : "+v"(acc) : "v"(a), "a"(b))
#define MFENCE1(a) asm volatile("s_nop 7\n\ts_nop 3" : "+v"(a))
#define MFENCE2(a, b) asm volatile("s_nop 7\n\ts_nop 3" : "+v"(a), "+v"(b))
#define OPAQUE(x) asm volatile("" : "+v"(x))
#define DRAIN() asm volatile("s_waitcnt vmcnt(0)" ::: "memory")
#define SCHED() __builtin_amdgcn_sched_barrier(0)
#define BAR() do { asm volatile("s_waitcnt lgkmcnt(0)" ::: "memory"); __builtin_amdgcn_s_barrier(); asm volatile("" ::: "memory"); } while (0)

DI v4u lds128(LAS unsigned char* L, int off) { return *(LAS v4u*)(L + off); }
DI void sts128(LAS unsigned char* L, int off, v4u v) { *(LAS v4u*)(L + off) = v; }
DI v4u trfrag(LAS unsigned char* L, int a0, int a1) {
  s16x4 lo = __builtin_amdgcn_ds_read_tr16_b64_v4i16((LAS s16x4*)(L + a0));
  s16x4 hi = __builtin_amdgcn_ds_read_tr16_b64_v4i16((LAS s16x4*)(L + a1));
  v2u l = __builtin_bit_cast(v2u, lo), h = __builtin_bit_cast(v2u, hi);
  return v4u{l.x, l.y, h.x, h.y};
}
DI void st128(v4u v, rsrc_t rs, int vo, int so) {
  __builtin_amdgcn_raw_buffer_store_b128(v, rs, vo, so, AUX_SC1);
  asm volatile("s_nop 1" :: "v"(v) : "memory");
}
DI v4u ld128(rsrc_t rs, int vo, int so) { return __builtin_amdgcn_raw_buffer_load_b128(rs, vo, so, AUX_SC1); }

DI void block_map(int b, int& mt, int& hp) {
  const int x = b & 7, i = b >> 3;
  mt = 4 * (x >> 1) + (i & 3);
  hp = 8 * (x & 1) + (i >> 2);
}

DI void tile_store(const v2u (&in)[8], rsrc_t rs, int off) {
#pragma unroll
  for (int e = 0; e < 4; ++e) {
    v4u o;
#pragma unroll
    for (int d = 0; d < 4; ++d)
      o[d] = __builtin_amdgcn_perm(e < 2 ? in[2 * d + 1].x : in[2 * d + 1].y, e < 2 ? in[2 * d].x : in[2 * d].y, (e & 1) ? 0x07060302u : 0x05040100u);
    st128(o, rs, off + e * 16, 0);
  }
}
DI void w_load(const u16* W, int j, int hp, int t, v2u (&in)[8]) {
  const int g5 = t / 30, ch = t % 30;
  const u16* src = W + (40 * j + 8 * g5) * 1920 + (ch / 10) * 640 + hp * 40 + (ch % 10) * 4;
#pragma unroll
  for (int q = 0; q < 8; ++q) in[q] = *(const v2u*)(src + q * 1920);
}
DI void w_store(const v2u (&in)[8], rsrc_t rs, LAS unsigned char* L, int type, int j, int hp, int t) {
  const int g5 = t / 30, ch = t % 30, sec = ch / 10, hh = (ch % 10) / 5, cc = ch % 5;
  const int g = 5 * j + g5, ks = g >> 1, kh = g & 1, tl = sec == 1 ? 0 : (sec == 2 ? 1 : 2);
  const int off = cc < 4 ? tl * 40960 + ks * 1024 + (kh * 32 + 8 * cc + 4 * hh) * 16 : L_T3 + ks * 768 + (kh * 24 + 8 * tl + 4 * hh) * 16;
  tile_store(in, rs, WT_OFF + (type * 16 + hp) * WT_IMG + off);
  f32x4 s = {0.f, 0.f, 0.f, 0.f};
#pragma unroll
  for (int q = 0; q < 8; ++q) s += f32x4{bflo(in[q].x), bfhi(in[q].x), bflo(in[q].y), bfhi(in[q].y)};
  const int idx = cc < 4 ? tl * 32 + 8 * cc + 4 * hh : 96 + 8 * tl + 4 * hh;
  *(LAS f32x4*)(L + (type * 5 + g5) * 512 + idx * 4) = s;
}
DI void cs_store(rsrc_t rs, LAS unsigned char* L, int type, int j, int hp, int i) {
  float s = 0.f;
#pragma unroll
  for (int g5 = 0; g5 < 5; ++g5) s += *(LAS float*)(L + (type * 5 + g5) * 512 + i * 4);
  __builtin_amdgcn_raw_buffer_store_b32(__float_as_uint(s), rs, i * 4, CS_OFF + ((type * 16 + hp) * 16 + j) * 512, AUX_SC1);
}
DI void wo_load(const u16* Wo, int nq, int f, int sub, v2u (&in)[8]) {
  const int u = f % 10, tl = (f / 10) % 5, w = f / 50, kh = sub >> 3;
  const int t = tl == 0 ? 4 : ((w + tl) & 3);
  const u16* src = Wo + 160 * nq + 32 * t + 4 * (sub & 7);
#pragma unroll
  for (int q = 0; q < 8; ++q) {
    const int hq = u < 8 ? 4 * w + (u >> 1) : 4 * w + 2 * (u - 8) + (q >> 2);
    const int e = u < 8 ? 8 * (u & 1) + q : 16 + (q & 3);
    in[q] = *(const v2u*)(src + ((2 * hq + kh) * 20 + e) * 640);
  }
}
DI int wo_off(int f, int sub) {
  const int q3 = sub & 7, kh = sub >> 3, c = ((q3 >> 2) << 1) | (q3 & 1), h = (q3 >> 1) & 1;
  return f * 1024 + (kh * 32 + 8 * c + 4 * h) * 16;
}
DI void cs_build(rsrc_t rs, LAS unsigned char* L, int type, int hp, int i) {
  float v[16];
#pragma unroll
  for (int j = 0; j < 16; ++j) v[j] = __uint_as_float(__builtin_amdgcn_raw_buffer_load_b32(rs, i * 4 + j * 512, CS_OFF + (type * 16 + hp) * 8192, AUX_SC1));
  SCHED();
  float s = 0.f;
#pragma unroll
  for (int j = 0; j < 16; ++j) s += v[j];
  const float hi = rbf(s), r1 = s - hi, mid = rbf(r1), lo = r1 - mid;
  *(LAS v2u*)(L + L_CS + type * 1024 + i * 8) = v2u{pk2(hi, mid), pk2(lo, 0.f)};
}

DI void dma2k(const unsigned char* gsrc, unsigned ldsoff, unsigned lane16) {
  unsigned keep;
  asm volatile(
      "s_mov_b32 %0, m0\n\ts_mov_b32 m0, %2\n\ts_nop 3\n\t"
      "global_load_lds_dwordx4 %1, %3 sc1\n\tglobal_load_lds_dwordx4 %1, %3 offset:1024 sc1\n\t"
      "s_mov_b32 m0, %0"
      : "=&s"(keep) : "v"(lane16), "s"(ldsoff), "s"(gsrc) : "memory");
}
DI void dma_range(const unsigned char* gimg, unsigned ldsbase, int g0, int g1, int first, int step, unsigned lane16) {
  for (int g = g0 + first; g < g1; g += step) dma2k(gimg + g * 2048, ldsbase + g * 2048, lane16);
}

DI void publish(unsigned char* ws, int type, int mt, int hp, int tid) {
  if (tid < 16) {
    const int cu = type == 1 ? tid * 16 + hp : mt * 16 + tid;
    __hip_atomic_fetch_add((gu32*)(ws + CT_OFF + (type * 256 + cu) * 256), 1u, RLX_AGENT);
  }
}
DI void wait_ctr(unsigned char* ws, int type, int mt, int hp, unsigned val, int w) {
  if (w == 0) {
    gu32* f = (gu32*)(ws + CT_OFF + (type * 256 + mt * 16 + hp) * 256);
    for (unsigned sp = 0; sp < SPIN_CAP; ++sp)
      if (__builtin_amdgcn_readfirstlane(__hip_atomic_load(f, RLX_AGENT)) >= val) break;
    __builtin_amdgcn_fence(__ATOMIC_ACQUIRE, "wavefront");
  }
  BAR();
}

#define STAMP(i) do {} while (0)

template <class H> DI void phiA(const f32x2 (&a)[10], float rs, unsigned (&o)[10], H hook) {
  const f32x2 rs2 = {rs, rs}, rl2 = {rs * LOG2E, rs * LOG2E}, eps = {1e-6f, 1e-6f};
  f32x2 e[10];
#pragma unroll
  for (int i = 0; i < 10; ++i) e[i] = a[i] * rl2;
  hook(0); SCHED();
#pragma unroll
  for (int i = 0; i < 5; ++i) { e[i].x = exp2c(e[i].x); e[i].y = exp2c(e[i].y); }
  hook(1); SCHED();
#pragma unroll
  for (int i = 5; i < 10; ++i) { e[i].x = exp2c(e[i].x); e[i].y = exp2c(e[i].y); }
  hook(2); SCHED();
#pragma unroll
  for (int i = 0; i < 10; ++i) e[i] = e[i] + eps;
  SCHED();
  f32x2 t[10];
#pragma unroll
  for (int i = 0; i < 10; ++i) t[i] = __builtin_elementwise_fma(a[i], rs2, e[i]);
  hook(3); SCHED();
#pragma unroll
  for (int i = 0; i < 10; ++i) { t[i].x = fmaxf(t[i].x, e[i].x); t[i].y = fmaxf(t[i].y, e[i].y); }
  hook(4); SCHED();
#pragma unroll
  for (int i = 0; i < 10; ++i) o[i] = pkv(t[i]);
  hook(5); SCHED();
}
DI void phiB_mm(const v4u (&wf)[6], const f32x2 (&a)[10], float rs, f32x16& d0, f32x16& d1) {
  const f32x2 rs2 = {rs, rs};
  f32x2 t[10];
#pragma unroll
  for (int i = 0; i < 10; ++i) t[i] = a[i] * rs2;
  SCHED();
  const v4u b0 = {pkv(t[0]), pkv(t[1]), pkv(t[2]), pkv(t[3])};
  const v4u b1 = {pkv(t[4]), pkv(t[5]), pkv(t[6]), pkv(t[7])};
  const v4u b2 = {pkv(t[8]), pkv(t[9]), 0u, 0u};
  SCHED();
  MFMA0N(d0, wf[0], b0); MFMA0(d1, wf[3], b0);
  MFMA(d0, wf[1], b1);   MFMA(d1, wf[4], b1);
  MFMA(d0, wf[2], b2);   MFMA(d1, wf[5], b2);
}
template <class H> DI void phiB_sm(const f32x16& d0, const f32x16& d1, unsigned (&pos)[10], unsigned (&neg)[10], H hook) {
  f32x2 pv[10];
#pragma unroll
  for (int i = 0; i < 8; ++i) pv[i] = mk2(d0[2 * i], d0[2 * i + 1]);
  pv[8] = mk2(d1[0], d1[1]); pv[9] = mk2(d1[2], d1[3]);
  float mx0, mn0, mx1, mn1;
  asm("v_max3_f32 %0, %4, %5, %6\n\tv_min3_f32 %1, %4, %5, %6\n\tv_max3_f32 %2, %7, %8, %9\n\tv_min3_f32 %3, %7, %8, %9\n\t"
      "v_max3_f32 %0, %0, %10, %11\n\tv_min3_f32 %1, %1, %10, %11\n\tv_max3_f32 %2, %2, %12, %13\n\tv_min3_f32 %3, %3, %12, %13\n\t"
      "v_max3_f32 %0, %0, %14, %15\n\tv_min3_f32 %1, %1, %14, %15\n\tv_max3_f32 %2, %2, %16, %17\n\tv_min3_f32 %3, %3, %16, %17\n\t"
      "v_max3_f32 %0, %0, %18, %19\n\tv_min3_f32 %1, %1, %18, %19\n\tv_max3_f32 %2, %2, %20, %21\n\tv_min3_f32 %3, %3, %20, %21\n\t"
      "v_max3_f32 %0, %0, %22, %23\n\tv_min3_f32 %1, %1, %22, %23\n\tv_max_f32 %0, %0, %2\n\tv_min_f32 %1, %1, %3"
      : "=&v"(mx0), "=&v"(mn0), "=&v"(mx1), "=&v"(mn1)
      : "v"(pv[0].x), "v"(pv[0].y), "v"(pv[1].x), "v"(pv[1].y), "v"(pv[2].x), "v"(pv[2].y), "v"(pv[3].x), "v"(pv[3].y), "v"(pv[4].x), "v"(pv[4].y),
        "v"(pv[5].x), "v"(pv[5].y), "v"(pv[6].x), "v"(pv[6].y), "v"(pv[7].x), "v"(pv[7].y), "v"(pv[8].x), "v"(pv[8].y), "v"(pv[9].x), "v"(pv[9].y));
  const float mxl = -mx0 * LOG2E, mnl = mn0 * LOG2E;
  const f32x2 l2 = {LOG2E, LOG2E}, nl2 = {-LOG2E, -LOG2E}, mx2 = {mxl, mxl}, mn2 = {mnl, mnl};
  f32x2 ea[10], eb[10];
  hook(0); SCHED();
#pragma unroll
  for (int i = 0; i < 10; ++i) { ea[i] = __builtin_elementwise_fma(pv[i], l2, mx2); eb[i] = __builtin_elementwise_fma(pv[i], nl2, mn2); }
  hook(1); SCHED();
#pragma unroll
  for (int i = 0; i < 10; ++i) {
    ea[i].x = __builtin_amdgcn_exp2f(ea[i].x); ea[i].y = __builtin_amdgcn_exp2f(ea[i].y);
    eb[i].x = __builtin_amdgcn_exp2f(eb[i].x); eb[i].y = __builtin_amdgcn_exp2f(eb[i].y);
    if (i == 4) { hook(2); SCHED(); }
  }
  hook(3); SCHED();
  f32x2 sa0 = ea[0] + ea[1], sb0 = eb[0] + eb[1], sa1 = ea[2] + ea[3], sb1 = eb[2] + eb[3];
  SCHED();
#pragma unroll
  for (int i = 4; i < 10; i += 2) { sa0 += ea[i]; sb0 += eb[i]; sa1 += ea[i + 1]; sb1 += eb[i + 1]; SCHED(); }
  sa0 += sa1; sb0 += sb1;
  const float ra = __builtin_amdgcn_rcpf(sa0.x + sa0.y), rb = __builtin_amdgcn_rcpf(sb0.x + sb0.y);
  const f32x2 ra2 = {ra, ra}, rb2 = {rb, rb};
  hook(4); SCHED();
#pragma unroll
  for (int i = 0; i < 10; ++i) { ea[i] *= ra2; eb[i] *= rb2; }
  hook(5); SCHED();
#pragma unroll
  for (int i = 0; i < 10; ++i) { pos[i] = pkv(ea[i]); neg[i] = pkv(eb[i]); }
  SCHED();
}

DI void store_x(const f32x16& xr, const float (&xr4)[8], rsrc_t rsW, int l, int tt2, int nq, int w, int lane) {
  const int vo = XB_OFF + l * XB_SZ + tt2 * 40960 + nq * 10240;
  st128(v4u{pk2(xr[0], xr[1]), pk2(xr[2], xr[3]), pk2(xr[4], xr[5]), pk2(xr[6], xr[7])}, rsW, lane * 16, vo + 2 * w * 1024);
  st128(v4u{pk2(xr[8], xr[9]), pk2(xr[10], xr[11]), pk2(xr[12], xr[13]), pk2(xr[14], xr[15])}, rsW, lane * 16 + 1024, vo + 2 * w * 1024);
  f32x2 s0 = mk2(xr[0], xr[1]), q0 = s0 * s0, s1 = mk2(xr[2], xr[3]), q1 = s1 * s1;
  if (w < 2) {
    st128(v4u{pk2(xr4[0], xr4[1]), pk2(xr4[2], xr4[3]), pk2(xr4[4], xr4[5]), pk2(xr4[6], xr4[7])}, rsW, lane * 16, vo + (8 + w) * 1024);
#pragma unroll
    for (int i = 0; i < 4; i += 2) {
      const f32x2 v = mk2(xr4[2 * i], xr4[2 * i + 1]), u = mk2(xr4[2 * i + 2], xr4[2 * i + 3]);
      s0 += v; q0 = __builtin_elementwise_fma(v, v, q0); s1 += u; q1 = __builtin_elementwise_fma(u, u, q1);
    }
  }
#pragma unroll
  for (int i = 2; i < 8; i += 2) {
    const f32x2 v = mk2(xr[2 * i], xr[2 * i + 1]), u = mk2(xr[2 * i + 2], xr[2 * i + 3]);
    s0 += v; q0 = __builtin_elementwise_fma(v, v, q0); s1 += u; q1 = __builtin_elementwise_fma(u, u, q1);
  }
  s0 += s1; q0 += q1;
  const auto sw = __builtin_amdgcn_permlane32_swap(__float_as_uint(s0.x + s0.y), __float_as_uint(q0.x + q0.y), false, false);
  const float t = __uint_as_float(sw[0]) + __uint_as_float(sw[1]);
  __builtin_amdgcn_raw_buffer_store_b32(__float_as_uint(t), rsW, lane * 4, SB_OFF + l * SB_SZ + (tt2 * 16 + nq * 4 + w) * 256, AUX_SC1);
}

constexpr int XW = 16;

__global__ void __launch_bounds__(256, 1) main_kernel(Params p) {
  extern __shared__ __attribute__((aligned(16))) unsigned char smem[];
  LAS unsigned char* L = (LAS unsigned char*)smem;
  const int tid0 = threadIdx.x, w = __builtin_amdgcn_readfirstlane(tid0 >> 6);
  int mt, hp;
  block_map(blockIdx.x, mt, hp);
  unsigned char* ws = p.ws;
  const rsrc_t rsW = __builtin_amdgcn_make_buffer_rsrc((void*)ws, 0, WS_BYTES, 0x00020000);
  const unsigned ldsbase = (unsigned)(size_t)L;
  const int tt = 4 * mt + w;
  const int tt2 = 4 * mt + (hp >> 2), nq = hp & 3;
  f32x16 xr;
  float xr4[8];
  u64 tag;
  {
    const int tid = tid0, lane = tid & 63, h = lane >> 5, r = lane & 31, cu = mt * 16 + hp;
    STAMP(0);
    tag = __hip_atomic_load((gu64*)(ws + EP_OFF), RLX_AGENT) + 1ull;
    const unsigned char* xs = (const unsigned char*)p.x0 + (32 * tt2 + r) * 1280 + h * 16 + nq * 320;
    const v4u a = *(const v4u*)(xs + w * 64), b = *(const v4u*)(xs + w * 64 + 32), c4 = *(const v4u*)(xs + 256 + (w & 1) * 32);
    v2u iwa[8], ioa[8], iwb[8], iob[8];
    const int ta = tid < 150 ? tid : 149, to = mt * 200 + (tid >= 56 ? tid - 56 : 0);
    const int tb = tid >= 106 ? tid - 106 : 0, tc = cu * 50 + (tid < 50 ? tid : 49);
    w_load(p.wA, mt, hp, ta, iwa);
    wo_load(p.woA, nq, to >> 4, to & 15, ioa);
    SCHED();
    w_load(p.wB, mt, hp, tb, iwb);
    wo_load(p.woB, tc / 3200, (tc % 3200) >> 4, tc & 15, iob);
    unsigned wpv[16];
    const int c = r >> 3, hi = (r >> 2) & 1, e = r & 3;
#pragma unroll
    for (int i = 0; i < 2; ++i) {
      const int fr = (w + 4 * i) % 6, tile = fr / 3, s = fr % 3;
      const int f = tile == 0 ? 4 * c + e : 16 + e;
#pragma unroll
      for (int j = 0; j < 8; ++j) { const int d = 8 * s + j; wpv[8 * i + j] = p.wphi[((2 * hp + h) * 20 + (d < 20 ? d : 0)) * 20 + f]; }
    }
    if (tid < 3) __hip_atomic_store((gu32*)(ws + CT_OFF + (tid * 256 + cu) * 256), 0u, RLX_AGENT);
    SCHED();
#pragma unroll
    for (int i = 0; i < 4; ++i) {
      xr[2 * i] = bflo(a[i]); xr[2 * i + 1] = bfhi(a[i]);
      xr[8 + 2 * i] = bflo(b[i]); xr[8 + 2 * i + 1] = bfhi(b[i]);
      xr4[2 * i] = bflo(c4[i]); xr4[2 * i + 1] = bfhi(c4[i]);
    }
    store_x(xr, xr4, rsW, 0, tt2, nq, w, lane);
    if (tid < 150) w_store(iwa, rsW, L, 0, mt, hp, ta);
    if (tid >= 56) tile_store(ioa, rsW, WOA_OFF + hp * 204800 + wo_off(to >> 4, to & 15));
    BAR();
    if (tid < 120) cs_store(rsW, L, 0, mt, hp, tid);
    DRAIN();
    BAR();
    if (tid < 32) {
      const int dst = tid < 16 ? mt * 16 + tid : (tid - 16) * 16 + hp, slot = tid < 16 ? hp : 16 + mt;
      __hip_atomic_store((gu64*)(ws + TG_OFF + dst * 256 + slot * 8), tag, RLX_AGENT);
    }
    STAMP(61);
    if (tid >= 106) w_store(iwb, rsW, L, 1, mt, hp, tb);
    if (tid < 50) tile_store(iob, rsW, WOB_OFF + (tc / 3200) * 204800 + wo_off((tc % 3200) >> 4, tc & 15));
#pragma unroll
    for (int i = 0; i < 2; ++i) {
      const int fr = (w + 4 * i) % 6, tile = fr / 3, s = fr % 3;
      const bool rowok = (hi == h) && (tile == 0 || c == 0);
      unsigned v[8];
#pragma unroll
      for (int j = 0; j < 8; ++j) v[j] = (rowok && 8 * s + j < 20) ? wpv[8 * i + j] : 0u;
      sts128(L, L_WPH + fr * 1024 + lane * 16, v4u{v[0] | (v[1] << 16), v[2] | (v[3] << 16), v[4] | (v[5] << 16), v[6] | (v[7] << 16)});
    }
    if (tid < 5) sts128(L, L_ZERO + tid * 16, v4u{0u, 0u, 0u, 0u});
    BAR();
    if (tid >= 128 && tid < 248) cs_store(rsW, L, 1, mt, hp, tid - 128);
    if (w == 0) {
      gu64* f = (gu64*)(ws + TG_OFF + cu * 256 + r * 8);
      for (unsigned sp = 0; sp < SPIN_CAP; ++sp)
        if (__all(__hip_atomic_load(f, RLX_AGENT) == tag)) break;
      __builtin_amdgcn_fence(__ATOMIC_ACQUIRE, "wavefront");
    }
    BAR();
    STAMP(62);
    const unsigned char* w0 = ws + WT_OFF + hp * WT_IMG;
    dma_range(w0, ldsbase, 0, 40, w, 4, lane * 16);
    dma_range(w0, ldsbase, 60, 75, w, 4, lane * 16);
    if (tid < 128) cs_build(rsW, L, 0, hp, tid);
    DRAIN();
    dma_range(w0, ldsbase, 40, 60, w, 4, lane * 16);
    BAR();
  }

  for (int l = 0; l < NLAYERS; ++l) {
    int tid = tid0;
    OPAQUE(tid);
    const int lane = tid & 63, h = lane >> 5, r = lane & 31, lane16 = lane * 16;
    const bool isB = l & 1;
    const int xso = XB_OFF + l * XB_SZ + tt * 40960;
    const int sso = SB_OFF + l * SB_SZ + tt * 4096;
    const int wov = (isB ? WOB_OFF + nq * 204800 : WOA_OFF + (l == 0 ? hp : nq) * 204800) + w * 51200;
    int aq = L_Q + lane16, at = L_T3 + (h * 24 + (r < 24 ? r : 23)) * 16, ak = lane16, av = L_V + lane16, am = L_WPH + lane16;
    OPAQUE(aq); OPAQUE(at); OPAQUE(ak); OPAQUE(av); OPAQUE(am);
    STAMP(1 + l * 12 + 0);
    v4u xf[40], wo[50];
#define LDX(ks) ld128(rsW, lane16 + ((ks) & 3) * 1024, xso + ((ks) >> 2) * 4096)
#define LDWO(i) wo[i] = ld128(rsW, lane16 + ((i) & 3) * 1024, wov + ((i) >> 2) * 4096)
#pragma unroll
    for (int ks = 0; ks < XW; ++ks) { xf[ks] = LDX(ks); SCHED(); }
    f32x16 aK, aV, aQ, aT;
    float rs;
    v4u mb, csq;
    {
      v4u fk[3], fv[3], ft[3];
      v2u cs[4];
      float sp[16];
#pragma unroll
      for (int ks = 0; ks < 2; ++ks) { fk[ks] = lds128(L, ak + ks * 1024); ft[ks] = lds128(L, at + ks * 768); fv[ks] = lds128(L, av + ks * 1024); }
      SCHED();
      float mean = 0.f, var = 0.f;
#pragma unroll
      for (int ks = 0; ks < 40; ++ks) {
        const int c = ks % 3, n = (ks + 2) % 3;
        if (ks == 0) MFMA0B(aK, fk[c], xf[ks]); else MFMAB(aK, fk[c], xf[ks]);
        if (ks + 2 < 40) fk[n] = lds128(L, ak + (ks + 2) * 1024);
        if (ks + XW < 40) xf[ks + XW] = LDX(ks + XW);
        if (ks >= 30) LDWO(ks - 30);
        SCHED();
        if (ks == 0) MFMA0B(aT, ft[c], xf[ks]); else MFMAB(aT, ft[c], xf[ks]);
        if (ks + 2 < 40) ft[n] = lds128(L, at + (ks + 2) * 768);
        if (ks < 16) sp[ks] = __uint_as_float(__builtin_amdgcn_raw_buffer_load_b32(rsW, lane * 4 + ks * 256, sso, AUX_SC1));
        if (ks == 30) {
#pragma unroll
          for (int i = 0; i < 4; ++i) cs[i] = *(LAS v2u*)(L + L_CS + (l & 1) * 1024 + i * 256 + r * 8);
        }
        if (ks == 31) { sp[0] += sp[1]; sp[2] += sp[3]; sp[4] += sp[5]; sp[6] += sp[7]; }
        if (ks == 32) { sp[0] += sp[2]; sp[4] += sp[6]; sp[8] += sp[10]; sp[12] += sp[14]; }
        if (ks == 33) {
          const auto sw = __builtin_amdgcn_permlane32_swap(__float_as_uint(sp[0]), __float_as_uint(sp[0]), false, false);
          mean = __uint_as_float(sw[0]) * (1.f / 640.f); var = __uint_as_float(sw[1]);
        }
        if (ks == 34) { const unsigned nm = h ? 0u : pk2(-mean, -mean); mb = v4u{nm, nm, 0u, 0u}; }
        if (ks == 35) { var = __builtin_amdgcn_sqrtf(var); var = rbf(var); }
        SCHED();
        if (ks == 0) MFMA0B(aV, fv[c], xf[ks]); else MFMAB(aV, fv[c], xf[ks]);
        if (ks + 2 < 40) fv[n] = lds128(L, av + (ks + 2) * 1024);
        if (ks == 31) { sp[8] += sp[9]; sp[10] += sp[11]; sp[12] += sp[13]; sp[14] += sp[15]; }
        if (ks == 32) { sp[0] += sp[4]; sp[8] += sp[12]; sp[0] += sp[8]; }
        if (ks == 33) { var = fmaxf(fmaf(var, 1.f / 640.f, -mean * mean), 0.f); }
        if (ks == 34) { var = rbf(var) + 1e-5f; var = rbf(var); }
        if (ks == 35) { rs = __builtin_amdgcn_rcpf(var); }
        SCHED();
        if (ks == 36) {
          const v4u c0 = {cs[0].x, cs[0].y, 0u, 0u}, c1 = {cs[1].x, cs[1].y, 0u, 0u}, c3 = {cs[3].x, cs[3].y, 0u, 0u};
          csq = v4u{cs[2].x, cs[2].y, 0u, 0u};
          MFMAN(aK, c0, mb); MFMAN(aT, c3, mb); MFMAN(aV, c1, mb);
          SCHED();
        }
      }
    }
    STAMP(1 + l * 12 + 1);
    {
      unsigned pos[10];
      f32x2 a[10];
      const f32x2 rs2 = {rs, rs};
      const int row = h * 8192 + (32 * w + r) * 64, sw = (r >> 1) & 3;
      const int u0 = row + (0 ^ sw) * 16, u1 = row + (1 ^ sw) * 16, u2 = row + (2 ^ sw) * 16;
      v4u wf[6];
#pragma unroll
      for (int i = 0; i < 6; ++i) wf[i] = lds128(L, am + i * 1024);
      SCHED();
      asm volatile("s_nop 5" : "+v"(aK), "+v"(aT), "+v"(aV));
#pragma unroll
      for (int i = 0; i < 8; ++i) a[i] = mk2(aK[2 * i], aK[2 * i + 1]);
      a[8] = mk2(aT[0], aT[1]); a[9] = mk2(aT[2], aT[3]);
#define VPART() do { \
        f32x2 t[10]; \
        _Pragma("unroll") for (int i = 0; i < 8; ++i) t[i] = mk2(aV[2 * i], aV[2 * i + 1]) * rs2; \
        t[8] = mk2(aT[4], aT[5]) * rs2; t[9] = mk2(aT[6], aT[7]) * rs2; \
        LDWO(10); SCHED(); \
        unsigned vv[10]; \
        _Pragma("unroll") for (int i = 0; i < 10; ++i) vv[i] = pkv(t[i]); \
        LDWO(11); SCHED(); \
        BAR();     \
        sts128(L, L_IV + u0, v4u{vv[0], vv[1], vv[2], vv[3]}); \
        sts128(L, L_IV + u1, v4u{vv[4], vv[5], vv[6], vv[7]}); \
        sts128(L, L_IV + u2, v4u{vv[8], vv[9], 0x00003f80u, 0u}); \
      } while (0)
      auto hk = [&](int n) __attribute__((always_inline)) {
        if (n == 0) { LDWO(12); LDWO(13); } if (n == 1) { LDWO(14); LDWO(15); } if (n == 2) { LDWO(16); LDWO(17); }
        if (n == 3) { LDWO(18); } if (n == 4) { LDWO(19); }
      };
      if (!isB) {
        VPART();
        phiA(a, rs, pos, hk);
      } else {
        f32x16 d0, d1;
        unsigned neg[10];
        phiB_mm(wf, a, rs, d0, d1);
        VPART();
        OPAQUE(d0); OPAQUE(d1);
        phiB_sm(d0, d1, pos, neg, hk);
        sts128(L, L_IPK1 + u0, v4u{neg[0], neg[1], neg[2], neg[3]});
        sts128(L, L_IPK1 + u1, v4u{neg[4], neg[5], neg[6], neg[7]});
        sts128(L, L_IPK1 + u2, v4u{neg[8], neg[9], 0u, 0u});
      }
      sts128(L, L_IPK0 + u0, v4u{pos[0], pos[1], pos[2], pos[3]});
      sts128(L, L_IPK0 + u1, v4u{pos[4], pos[5], pos[6], pos[7]});
      sts128(L, L_IPK0 + u2, v4u{pos[8], pos[9], 0u, 0u});
      BAR();
    }
    STAMP(1 + l * 12 + 2);
    const int nroles = isB ? 4 : 2;
    const int kvp = KVP_OFF + l * KVP_SZ + hp * 16 * KVP_ONE;
    if (w < nroles) {
      const int head = w & 1, part = w >> 1;
      const int g = lane >> 4, kh = g >> 1, blk = g & 1, q = (lane & 15) >> 2, pp = lane & 3;
      const int un = 2 * blk + (pp >> 1);
      const int a0 = head * 8192 + (8 * kh + q) * 64 + ((un ^ (q >> 1)) * 16) + 8 * (pp & 1);
      const int a1 = head * 8192 + (8 * kh + 4 + q) * 64 + ((un ^ (2 + (q >> 1))) * 16) + 8 * (pp & 1);
      const int pb = part * 16384;
      v4u fa[8], fb[8];
#pragma unroll
      for (int s = 0; s < 8; ++s) {
        fa[s] = trfrag(L, pb + a0 + s * 1024, pb + a1 + s * 1024);
        fb[s] = trfrag(L, L_IV + a0 + s * 1024, L_IV + a1 + s * 1024);
        SCHED();
      }
      f32x16 d;
      MFMA0(d, fa[0], fb[0]);
#pragma unroll
      for (int s = 1; s < 8; ++s) MFMA(d, fa[s], fb[s]);
      MFENCE1(d);
      if (r < 21) {
        const v4u ua = {pk2(d[0], d[1]), pk2(d[2], d[3]), pk2(d[4], d[5]), pk2(d[6], d[7])};
        st128(ua, rsW, ((w * 2 + h) * 21 + r) * 16, kvp + mt * KVP_ONE);
        if (h == 0) {
          const v2u ub = {pk2(d[8], d[9]), pk2(d[10], d[11])};
          __builtin_amdgcn_raw_buffer_store_b64(ub, rsW, nroles * 672 + (w * 21 + r) * 8, kvp + mt * KVP_ONE, AUX_SC1);
        }
      }
    }
    {
      constexpr int QD = 4, Q1 = 16;
      v4u fq[QD + 1];
#pragma unroll
      for (int ks = 0; ks < QD; ++ks) fq[ks] = lds128(L, aq + ks * 1024);
      SCHED();
      asm volatile("s_nop 3");
      MFMA0(aQ, csq, mb);
#pragma unroll
      for (int ks = 0; ks < 40; ++ks) {
        MFMAB(aQ, fq[ks % (QD + 1)], xf[ks]);
        if (ks + QD < 40) fq[(ks + QD) % (QD + 1)] = lds128(L, aq + (ks + QD) * 1024);
        if (ks >= Q1 && ((ks - Q1) % 4) != 3) LDWO(20 + (ks - Q1) - (ks - Q1) / 4);
        SCHED();
        if (ks == Q1 - 1) {
          DRAIN();
          __builtin_amdgcn_s_barrier();
          publish(ws, 1, mt, hp, tid);
          STAMP(1 + l * 12 + 3);
          asm volatile("s_nop 3");
          SCHED();
        }
      }
    }
    unsigned pq[20];
    {
      f32x2 a[10];
      unsigned pos[10], neg[10];
      v4u wf[6];
#pragma unroll
      for (int i = 0; i < 6; ++i) wf[i] = lds128(L, am + i * 1024);
      SCHED();
      asm volatile("s_nop 5" : "+v"(aQ));
#pragma unroll
      for (int i = 0; i < 8; ++i) a[i] = mk2(aQ[2 * i], aQ[2 * i + 1]);
      a[8] = mk2(aT[8], aT[9]); a[9] = mk2(aT[10], aT[11]);
      auto hk = [&](int n) __attribute__((always_inline)) {
        if (n == 0) { LDWO(38); LDWO(39); } if (n == 1) { LDWO(40); LDWO(41); } if (n == 2) { LDWO(42); LDWO(43); }
        if (n == 3) { LDWO(44); LDWO(45); } if (n == 4) { LDWO(46); LDWO(47); } if (n == 5) { LDWO(48); LDWO(49); }
      };
      if (!isB) {
        phiA(a, rs, pos, hk);
#pragma unroll
        for (int i = 0; i < 10; ++i) neg[i] = 0u;
      } else {
        f32x16 d0, d1;
        phiB_mm(wf, a, rs, d0, d1);
        MFENCE2(d0, d1);
        phiB_sm(d0, d1, pos, neg, hk);
      }
#pragma unroll
      for (int i = 0; i < 10; ++i) { pq[i] = pos[i]; pq[10 + i] = neg[i]; }
    }
    const int nA = 42 * nroles, nU = nA + (21 * nroles + 1) / 2;
    int d0a, d1a;
    if (tid < nA) {
      const int role = tid / 42, rem = tid % 42, hh = rem / 21, e = rem % 21;
      d0a = L_KVI + (role & 1) * KVI_HS + e * 96 + (role >> 1) * 40 + hh * 8;
      d1a = d0a + 16;
    } else {
      const int c0 = 2 * (tid - nA), c1 = c0 + 1;
      const int r0 = c0 / 21, e0 = c0 % 21, r1 = c1 / 21, e1 = c1 % 21;
      d0a = L_KVI + (r0 & 1) * KVI_HS + e0 * 96 + (r0 >> 1) * 40 + 32;
      d1a = L_KVI + (r1 & 1) * KVI_HS + e1 * 96 + (r1 >> 1) * 40 + 32;
    }
    int b0, b1;
    {
      const int c = r >> 3, hi = (r >> 2) & 1, e = r & 3;
      b0 = hi == h ? L_KVI + h * KVI_HS + (4 * c + e) * 96 : L_ZERO;
      b1 = hi == h ? L_KVI + h * KVI_HS + (c == 0 ? 16 + e : 20) * 96 : L_ZERO;
    }
    OPAQUE(d0a); OPAQUE(d1a); OPAQUE(b0); OPAQUE(b1);
    STAMP(1 + l * 12 + 4);
    wait_ctr(ws, 1, mt, hp, 16u * (l + 1), w);
    STAMP(1 + l * 12 + 5);
    if (tid < nU) {
      v4u pp[16];
#pragma unroll
      for (int m = 0; m < 16; ++m) { pp[m] = ld128(rsW, tid * 16, kvp + m * KVP_ONE); SCHED(); }
      f32x2 s[4];
#pragma unroll
      for (int m = 0; m < 16; ++m) {
#pragma unroll
        for (int i = 0; i < 4; ++i) {
          const f32x2 v = mk2(bflo(pp[m][i]), bfhi(pp[m][i]));
          if (m == 0) s[i] = v; else s[i] += v;
        }
        SCHED();
      }
      *(LAS v2u*)(L + d0a) = v2u{pkv(s[0]), pkv(s[1])};
      *(LAS v2u*)(L + d1a) = v2u{pkv(s[2]), pkv(s[3])};
    }
    BAR();
    STAMP(1 + l * 12 + 6);
    {
      v4u fa[5], fb[5];
#pragma unroll
      for (int s = 0; s < 5; ++s) { fb[s] = lds128(L, b1 + 16 * s); fa[s] = lds128(L, b0 + 16 * s); SCHED(); }
      f32x16 n0, n1;
      const v4u p0 = {pq[0], pq[1], pq[2], pq[3]}, p1 = {pq[4], pq[5], pq[6], pq[7]}, p2 = {pq[8], pq[9], pq[10], pq[11]};
      const v4u p3 = {pq[12], pq[13], pq[14], pq[15]}, p4 = {pq[16], pq[17], pq[18], pq[19]};
      MFMA0N(n1, fb[0], p0); MFMA0(n0, fa[0], p0);
      MFMA(n1, fb[1], p1); MFMA(n0, fa[1], p1);
      if (!isB) { fa[2].z = 0u; fa[2].w = 0u; fb[2].z = 0u; fb[2].w = 0u; }
      MFMAN(n1, fb[2], p2); MFMA(n0, fa[2], p2);
      if (isB) { MFMAN(n1, fb[3], p3); MFMA(n0, fa[3], p3); MFMA(n1, fb[4], p4); MFMA(n0, fa[4], p4); }
      MFENCE2(n0, n1);
      const float inv = __builtin_amdgcn_rcpf(n1[4] + 1e-6f);
      const f32x2 iv = {inv, inv};
      f32x2 t[10];
#pragma unroll
      for (int i = 0; i < 8; ++i) t[i] = mk2(n0[2 * i], n0[2 * i + 1]) * iv;
      t[8] = mk2(n1[0], n1[1]) * iv; t[9] = mk2(n1[2], n1[3]) * iv;
      SCHED();
      const v4u o0 = {pkv(t[0]), pkv(t[1]), pkv(t[2]), pkv(t[3])};
      const v4u o1 = {pkv(t[4]), pkv(t[5]), pkv(t[6]), pkv(t[7])};
      const v2u o2 = {pkv(t[8]), pkv(t[9])};
      const int ob = OB_OFF + l * OB_SZ + (tt * 16 + hp) * 2560;
      st128(o0, rsW, lane16, ob);
      st128(o1, rsW, lane16 + 1024, ob);
      __builtin_amdgcn_raw_buffer_store_b64(o2, rsW, lane * 8 + 2048, ob, AUX_SC1);
    }
    DRAIN();
    BAR();
    publish(ws, 2, mt, hp, tid);
    STAMP(1 + l * 12 + 7);
    const unsigned char* wnext = ws + WT_OFF + (((l + 1) & 1) * 16 + hp) * WT_IMG;
    if (l == 0 && tid >= 128) cs_build(rsW, L, 1, hp, tid - 128);
    if (l + 1 < NLAYERS && w > 0) dma_range(wnext, ldsbase, 0, 40, w - 1, 3, lane16);
    int rw0 = L_RED4 + w * 4096 + lane16, rw1 = L_RED + (((w + 1) & 3) * 3 + 2) * 4096 + lane16;
    int rw2 = L_RED + (((w + 2) & 3) * 3 + 1) * 4096 + lane16, rw3 = L_RED + (((w + 3) & 3) * 3) * 4096 + lane16;
    int rr0 = L_RED + w * 12288 + lane16, rr4 = L_RED4 + (w & 1) * 2048 + lane16;
    OPAQUE(rw0); OPAQUE(rw1); OPAQUE(rw2); OPAQUE(rw3); OPAQUE(rr0); OPAQUE(rr4);
    wait_ctr(ws, 2, mt, hp, 16u * (l + 1), w);
    STAMP(1 + l * 12 + 8);
    {
      v4u of[10];
      const int ob = OB_OFF + l * OB_SZ + (tt2 * 16 + 4 * w) * 2560;
      v2u oh[4];
#pragma unroll
      for (int a = 0; a < 4; ++a) {
        of[2 * a] = ld128(rsW, lane16, ob + a * 2560); SCHED();
        of[2 * a + 1] = ld128(rsW, lane16 + 1024, ob + a * 2560); SCHED();
      }
#pragma unroll
      for (int a = 0; a < 4; ++a) oh[a] = __builtin_amdgcn_raw_buffer_load_b64(rsW, lane * 8 + 2048, ob + a * 2560, AUX_SC1);
      SCHED();
      of[8] = v4u{oh[0].x, oh[0].y, oh[1].x, oh[1].y};
      of[9] = v4u{oh[2].x, oh[2].y, oh[3].x, oh[3].y};
      f32x16 acc[4];
      v4u rd[12], r4[8];
#pragma unroll
      for (int u = 0; u < 10; ++u) {
        if (u == 0) { MFMA0A(acc[0], wo[0], of[0]); MFMA0A(acc[1], wo[10], of[0]); }
        else {
          if (u == 8) asm volatile("s_nop 1" : "+v"(of[8]), "+v"(of[9]));
          MFMAA(acc[0], wo[u], of[u]); MFMAA(acc[1], wo[10 + u], of[u]);
        }
      }
#pragma unroll
      for (int tl = 2; tl < 5; ++tl) {
#pragma unroll
        for (int u = 0; u < 10; ++u) {
          if (tl < 4) { if (u == 0) MFMA0A(acc[tl], wo[tl * 10], of[0]); else MFMAA(acc[tl], wo[tl * 10 + u], of[u]); }
          else MFMA(xr, wo[tl * 10 + u], of[u]);
          const int st = tl == 2 ? (u < 6 ? 0 : 1) : tl - 1;
          const int q0 = tl == 2 ? (u < 6 ? u - 2 : u - 6) : (tl == 3 ? u - 2 : 2 * (u - 2));
          const int qn = tl == 4 ? 2 : 1;
#pragma unroll
          for (int qd = q0; qd < q0 + qn; ++qd)
            if (qd >= 0 && qd < 4) {
              if (qd == 0) OPAQUE(acc[st]);
              const int dst = st == 0 ? rw0 : (st == 1 ? rw1 : (st == 2 ? rw2 : rw3));
              sts128(L, dst + qd * 1024, __builtin_bit_cast(v4u, f32x4{acc[st][4 * qd], acc[st][4 * qd + 1], acc[st][4 * qd + 2], acc[st][4 * qd + 3]}));
            }
          if (tl == 4 && u == 5) BAR();
          if (tl == 4 && u >= 5 && u < 9) {
#pragma unroll
            for (int i = 0; i < 3; ++i) rd[(u - 5) * 3 + i] = lds128(L, rr0 + (i * 4 + (u - 5)) * 1024);
#pragma unroll
            for (int i = 0; i < 2; ++i) r4[(u - 5) * 2 + i] = lds128(L, rr4 + (u - 5) * 4096 + i * 1024);
          }
          SCHED();
        }
      }
      STAMP(1 + l * 12 + 9);
      f32x4 sm[4];
#pragma unroll
      for (int qd = 0; qd < 4; ++qd) sm[qd] = __builtin_bit_cast(f32x4, rd[qd * 3]) + __builtin_bit_cast(f32x4, rd[qd * 3 + 1]);
      SCHED();
#pragma unroll
      for (int qd = 0; qd < 4; ++qd) sm[qd] += __builtin_bit_cast(f32x4, rd[qd * 3 + 2]);
      SCHED();
      OPAQUE(xr);
#pragma unroll
      for (int i = 0; i < 16; ++i) xr[i] += sm[i >> 2][i & 3];
      {
        const f32x4 t0 = (__builtin_bit_cast(f32x4, r4[0]) + __builtin_bit_cast(f32x4, r4[2])) + (__builtin_bit_cast(f32x4, r4[4]) + __builtin_bit_cast(f32x4, r4[6]));
        const f32x4 t1 = (__builtin_bit_cast(f32x4, r4[1]) + __builtin_bit_cast(f32x4, r4[3])) + (__builtin_bit_cast(f32x4, r4[5]) + __builtin_bit_cast(f32x4, r4[7]));
#pragma unroll
        for (int i = 0; i < 4; ++i) { xr4[i] += t0[i]; xr4[4 + i] += t1[i]; }
      }
    }
    if (l + 1 < NLAYERS) {
      store_x(xr, xr4, rsW, l + 1, tt2, nq, w, lane);
      DRAIN();
      BAR();
      publish(ws, 0, mt, hp, tid);
      STAMP(1 + l * 12 + 10);
      if (w > 0) {
        dma_range(wnext, ldsbase, 60, 75, w - 1, 3, lane16);
        DRAIN();
        dma_range(wnext, ldsbase, 40, 60, w - 1, 3, lane16);
      }
      wait_ctr(ws, 0, mt, hp, 16u * (l + 1), w);
    }
  }
  {
    const int tid = tid0, lane = tid & 63, h = lane >> 5, r = lane & 31;
    unsigned char* o = (unsigned char*)p.out + (32 * tt2 + r) * 1280 + h * 16 + nq * 320;
    *(v4u*)(o + w * 64) = v4u{pk2(xr[0], xr[1]), pk2(xr[2], xr[3]), pk2(xr[4], xr[5]), pk2(xr[6], xr[7])};
    *(v4u*)(o + w * 64 + 32) = v4u{pk2(xr[8], xr[9]), pk2(xr[10], xr[11]), pk2(xr[12], xr[13]), pk2(xr[14], xr[15])};
    if (w < 2) *(v4u*)(o + 256 + w * 32) = v4u{pk2(xr4[0], xr4[1]), pk2(xr4[2], xr4[3]), pk2(xr4[4], xr4[5]), pk2(xr4[6], xr4[7])};
    if (blockIdx.x == 0 && tid == 0) __hip_atomic_store((gu64*)(ws + EP_OFF), tag, RLX_AGENT);
    STAMP(60);
  }
}

extern "C" void kernel_launch(void* const* d_in, const int* in_sizes, int n_in, void* d_out, int out_size, void* d_ws, size_t ws_size,
                              hipStream_t stream) {
  Params p;
  p.x0 = (const u16*)d_in[0]; p.wA = (const u16*)d_in[1]; p.woA = (const u16*)d_in[2];
  p.wB = (const u16*)d_in[4]; p.wphi = (const u16*)d_in[5]; p.woB = (const u16*)d_in[6];
  p.out = (u16*)d_out; p.ws = (unsigned char*)d_ws;
  (void)hipFuncSetAttribute((const void*)main_kernel, hipFuncAttributeMaxDynamicSharedMemorySize, L_BYTES);
  main_kernel<<<256, 256, L_BYTES, stream>>>(p);
}
```

```cpp
#include <hip/hip_runtime.h>
#include <stdint.h>

#define GAS __attribute__((address_space(1)))
#define LAS __attribute__((address_space(3)))
#define DI __device__ __forceinline__
#define RLX_AGENT __ATOMIC_RELAXED, __HIP_MEMORY_SCOPE_AGENT
typedef unsigned short u16;
typedef unsigned long long u64;
typedef GAS unsigned gu32;
typedef GAS u64 gu64;
typedef unsigned v4u __attribute__((ext_vector_type(4)));
typedef unsigned v2u __attribute__((ext_vector_type(2)));
typedef short s16x4 __attribute__((ext_vector_type(4)));
typedef __bf16 bf16x2 __attribute__((ext_vector_type(2)));
typedef float f32x2 __attribute__((ext_vector_type(2)));
typedef float f32x4 __attribute__((ext_vector_type(4)));
typedef float f32x16 __attribute__((ext_vector_type(16)));
typedef __amdgpu_buffer_rsrc_t rsrc_t;

#define NLAYERS 4
#define MAP 0
#define NOWO 0
#define NODMA 0
#define FINE 0
#define STAMPS 0

constexpr int EP_OFF = 0;
constexpr int TG_OFF = 256;
constexpr int CT_OFF = TG_OFF + 256 * 256;
constexpr int CS_OFF = CT_OFF + 3 * 256 * 256;
constexpr int SB_OFF = CS_OFF + 32 * 8192;
constexpr int SB_SZ = 64 * 16 * 256;
constexpr int KVP_OFF = SB_OFF + 4 * SB_SZ;
constexpr int KVP_ONE = 3456;
constexpr int KVP_SZ = 256 * KVP_ONE;
constexpr int XB_OFF = KVP_OFF + 4 * KVP_SZ;
constexpr int XB_SZ = 64 * 40960;
constexpr int OB_OFF = XB_OFF + 4 * XB_SZ;
constexpr int WT_OFF = OB_OFF + 4 * XB_SZ;
constexpr int WT_IMG = 153600;
constexpr int WOB_OFF = WT_OFF + 32 * WT_IMG;
constexpr int WOA_OFF = WOB_OFF + 4 * 204800;
constexpr int ST_OFF = WOA_OFF + 16 * 204800;
constexpr int WS_BYTES = ST_OFF + 256 * 64 * 8;
constexpr int CODE_LINES = 192;

constexpr int L_K = 0, L_V = 40960, L_Q = 81920, L_T3 = 122880;
constexpr int L_WPH = 153600;
constexpr int L_CS = L_WPH + 6144;
constexpr int L_ZERO = L_CS + 2048;
constexpr int L_BYTES = 163840;
constexpr int L_IPK0 = 0, L_IPK1 = 16384, L_IV = 32768;
constexpr int L_KVI = 49152;
constexpr int KVI_HS = 21 * 96;
constexpr int L_RED = L_Q;
constexpr int L_RED4 = L_Q + 49152;
constexpr int S_W = 0, S_WO = 20480, S_PH = 51200;

constexpr int AUX_SC1 = 16;
constexpr float LOG2E = 1.4426950408889634f;
constexpr unsigned SPIN_CAP = 1u << 17;

struct Params {
  const u16* x0; const u16* wA; const u16* woA; const u16* wB; const u16* wphi; const u16* woB;
  u16* out; unsigned char* ws;
};

DI f32x2 mk2(float a, float b) { return f32x2{a, b}; }
DI unsigned pkv(f32x2 v) { return __builtin_bit_cast(unsigned, __builtin_convertvector(v, bf16x2)); }
DI unsigned pk2(float a, float b) { return pkv(mk2(a, b)); }
DI float rbf(float a) { return __uint_as_float(pk2(a, a) << 16); }
DI float bflo(unsigned d) { return __uint_as_float(d << 16); }
DI float bfhi(unsigned d) { return __uint_as_float(d & 0xffff0000u); }
DI float exp2c(float x) { return __builtin_amdgcn_fmed3f(__builtin_amdgcn_exp2f(x), 0.f, 1.f); }

#define MF "v_mfma_f32_32x32x16_bf16 "
#define MFMA0(acc, a, b) asm volatile(MF "%0, %1, %2, 0" : "=&v"(acc) : "v"(a), "v"(b))
#define MFMA(acc, a, b) asm volatile(MF "%0, %1, %2, %0" : "+v"(acc) : "v"(a), "v"(b))
#define MFMA0N(acc, a, b) asm volatile("s_nop 1\n\t" MF "%0, %1, %2, 0" : "=&v"(acc) : "v"(a), "v"(b))
#define MFMAN(acc, a, b) asm volatile("s_nop 1\n\t" MF "%0, %1, %2, %0" : "+v"(acc) : "v"(a), "v"(b))
#define MFMA0A(acc, a, b) asm volatile(MF "%0, %1, %2, 0" : "=&v"(acc) : "a"(a), "v"(b))
#define MFMAA(acc, a, b) asm volatile(MF "%0, %1, %2, %0" : "+v"(acc) : "a"(a), "v"(b))
#define MFMA0B(acc, a, b) asm volatile(MF "%0, %1, %2, 0" : "=&v"(acc) : "v"(a), "a"(b))
#define MFMAB(acc, a, b) asm volatile(MF "%0, %1, %2, %0" : "+v"(acc) : "v"(a), "a"(b))
#define MFENCE1(a) asm volatile("s_nop 7\n\ts_nop 3" : "+v"(a))
#define MFENCE2(a, b) asm volatile("s_nop 7\n\ts_nop 3" : "+v"(a), "+v"(b))
#define OPAQUE(x) asm volatile("" : "+v"(x))
#define DRAIN() asm volatile("s_waitcnt vmcnt(0)" ::: "memory")
#define SCHED() __builtin_amdgcn_sched_barrier(0)
#define BAR() do { asm volatile("s_waitcnt lgkmcnt(0)" ::: "memory"); __builtin_amdgcn_s_barrier(); asm volatile("" ::: "memory"); } while (0)

template <int V> struct IC { static constexpr int value = V; };
template <int I, int N, class F> DI void sfor(F&& f) { if constexpr (I < N) { f(IC<I>{}); sfor<I + 1, N>(f); } }
#define LAMBDA(x) [&](auto x) __attribute__((always_inline))
DI v4u lds128(LAS unsigned char* L, int off) { return *(LAS v4u*)(L + off); }
DI void sts128(LAS unsigned char* L, int off, v4u v) { *(LAS v4u*)(L + off) = v; }
DI v4u trfrag(LAS unsigned char* L, int a0, int a1) {
  s16x4 lo = __builtin_amdgcn_ds_read_tr16_b64_v4i16((LAS s16x4*)(L + a0));
  s16x4 hi = __builtin_amdgcn_ds_read_tr16_b64_v4i16((LAS s16x4*)(L + a1));
  v2u l = __builtin_bit_cast(v2u, lo), h = __builtin_bit_cast(v2u, hi);
  return v4u{l.x, l.y, h.x, h.y};
}
DI void st128(v4u v, rsrc_t rs, int vo, int so) {
  __builtin_amdgcn_raw_buffer_store_b128(v, rs, vo, so, AUX_SC1);
  asm volatile("s_nop 1" :: "v"(v) : "memory");
}
DI v4u ld128(rsrc_t rs, int vo, int so) { return __builtin_amdgcn_raw_buffer_load_b128(rs, vo, so, AUX_SC1); }

DI void block_map(int b, int& mt, int& hp) {
  const int x = b & 7, i = b >> 3;
  mt = 4 * (x >> 1) + (i & 3);
  hp = 8 * (x & 1) + (i >> 2);
}

DI void dma1(const void* gbase, unsigned voff, unsigned ldsoff) {
  unsigned keep;
  asm volatile("s_mov_b32 %0, m0\n\ts_mov_b32 m0, %2\n\ts_nop 3\n\tglobal_load_lds_dwordx4 %1, %3\n\ts_mov_b32 m0, %0"
               : "=&s"(keep) : "v"(voff), "s"(ldsoff), "s"(gbase) : "memory");
}
DI void cs_build(rsrc_t rs, LAS unsigned char* L, int type, int hp, int lane) {
  v2u v[16];
#pragma unroll
  for (int j = 0; j < 16; ++j) v[j] = __builtin_amdgcn_raw_buffer_load_b64(rs, lane * 8 + j * 512, CS_OFF + (type * 16 + hp) * 8192, AUX_SC1);
  SCHED();
  f32x2 s = {0.f, 0.f};
#pragma unroll
  for (int j = 0; j < 16; ++j) s += __builtin_bit_cast(f32x2, v[j]);
  const float h0 = rbf(s.x), r0 = s.x - h0, m0 = rbf(r0), h1 = rbf(s.y), r1 = s.y - h1, m1 = rbf(r1);
  sts128(L, L_CS + type * 1024 + lane * 16, v4u{pk2(h0, m0), pk2(r0 - m0, 0.f), pk2(h1, m1), pk2(r1 - m1, 0.f)});
}
DI void wait_tags(unsigned char* ws, int off, u64 tag) {
  gu64* f = (gu64*)(ws + off);
  for (unsigned sp = 0; sp < SPIN_CAP; ++sp)
    if (__all(__hip_atomic_load(f, RLX_AGENT) == tag)) break;
  __builtin_amdgcn_fence(__ATOMIC_ACQUIRE, "wavefront");
}

DI void dma2k(const unsigned char* gsrc, unsigned ldsoff, unsigned lane16) {
  unsigned keep;
  asm volatile(
      "s_mov_b32 %0, m0\n\ts_mov_b32 m0, %2\n\ts_nop 3\n\t"
      "global_load_lds_dwordx4 %1, %3 sc1\n\tglobal_load_lds_dwordx4 %1, %3 offset:1024 sc1\n\t"
      "s_mov_b32 m0, %0"
      : "=&s"(keep) : "v"(lane16), "s"(ldsoff), "s"(gsrc) : "memory");
}
DI void dma_range(const unsigned char* gimg, unsigned ldsbase, int g0, int g1, int first, int step, unsigned lane16) {
  for (int g = g0 + first; g < g1; g += step) dma2k(gimg + g * 2048, ldsbase + g * 2048, lane16);
}

DI void publish(unsigned char* ws, int type, int mt, int hp, int tid) {
  if (tid < 16) {
    const int cu = type == 1 ? tid * 16 + hp : mt * 16 + tid;
    __hip_atomic_fetch_add((gu32*)(ws + CT_OFF + (type * 256 + cu) * 256), 1u, RLX_AGENT);
  }
}
DI unsigned poll_ctr(unsigned char* ws, int type, int mt, int hp) {
  return __hip_atomic_load((gu32*)(ws + CT_OFF + (type * 256 + mt * 16 + hp) * 256), RLX_AGENT);
}
DI void wait_ctr(unsigned char* ws, int type, int mt, int hp, unsigned val, unsigned seen) {
  for (unsigned sp = 0; sp < SPIN_CAP && __builtin_amdgcn_readfirstlane(seen) < val; ++sp) seen = poll_ctr(ws, type, mt, hp);
  __builtin_amdgcn_fence(__ATOMIC_ACQUIRE, "wavefront");
}

#define STAMP(i) do {} while (0)

template <class H> DI void phiA(const f32x2 (&a)[10], float rs, unsigned (&o)[10], H hook) {
  const f32x2 rs2 = {rs, rs}, rl2 = {rs * LOG2E, rs * LOG2E}, eps = {1e-6f, 1e-6f};
  f32x2 e[10];
#pragma unroll
  for (int i = 0; i < 10; ++i) e[i] = a[i] * rl2;
  hook(0); SCHED();
#pragma unroll
  for (int i = 0; i < 5; ++i) { e[i].x = exp2c(e[i].x); e[i].y = exp2c(e[i].y); }
  hook(1); SCHED();
#pragma unroll
  for (int i = 5; i < 10; ++i) { e[i].x = exp2c(e[i].x); e[i].y = exp2c(e[i].y); }
  hook(2); SCHED();
#pragma unroll
  for (int i = 0; i < 10; ++i) e[i] = e[i] + eps;
  SCHED();
  f32x2 t[10];
#pragma unroll
  for (int i = 0; i < 10; ++i) t[i] = __builtin_elementwise_fma(a[i], rs2, e[i]);
  hook(3); SCHED();
#pragma unroll
  for (int i = 0; i < 10; ++i) { t[i].x = fmaxf(t[i].x, e[i].x); t[i].y = fmaxf(t[i].y, e[i].y); }
  hook(4); SCHED();
#pragma unroll
  for (int i = 0; i < 10; ++i) o[i] = pkv(t[i]);
  hook(5); SCHED();
}
DI void phiB_mm(const v4u (&wf)[6], const f32x2 (&a)[10], float rs, f32x16& d0, f32x16& d1) {
  const f32x2 rs2 = {rs, rs};
  f32x2 t[10];
#pragma unroll
  for (int i = 0; i < 10; ++i) t[i] = a[i] * rs2;
  SCHED();
  const v4u b0 = {pkv(t[0]), pkv(t[1]), pkv(t[2]), pkv(t[3])};
  const v4u b1 = {pkv(t[4]), pkv(t[5]), pkv(t[6]), pkv(t[7])};
  const v4u b2 = {pkv(t[8]), pkv(t[9]), 0u, 0u};
  SCHED();
  MFMA0N(d0, wf[0], b0); MFMA0(d1, wf[3], b0);
  MFMA(d0, wf[1], b1);   MFMA(d1, wf[4], b1);
  MFMA(d0, wf[2], b2);   MFMA(d1, wf[5], b2);
}
template <class H> DI void phiB_sm(const f32x16& d0, const f32x16& d1, unsigned (&pos)[10], unsigned (&neg)[10], H hook) {
  f32x2 pv[10];
#pragma unroll
  for (int i = 0; i < 8; ++i) pv[i] = mk2(d0[2 * i], d0[2 * i + 1]);
  pv[8] = mk2(d1[0], d1[1]); pv[9] = mk2(d1[2], d1[3]);
  float mx0, mn0, mx1, mn1;
  asm("v_max3_f32 %0, %4, %5, %6\n\tv_min3_f32 %1, %4, %5, %6\n\tv_max3_f32 %2, %7, %8, %9\n\tv_min3_f32 %3, %7, %8, %9\n\t"
      "v_max3_f32 %0, %0, %10, %11\n\tv_min3_f32 %1, %1, %10, %11\n\tv_max3_f32 %2, %2, %12, %13\n\tv_min3_f32 %3, %3, %12, %13\n\t"
      "v_max3_f32 %0, %0, %14, %15\n\tv_min3_f32 %1, %1, %14, %15\n\tv_max3_f32 %2, %2, %16, %17\n\tv_min3_f32 %3, %3, %16, %17\n\t"
      "v_max3_f32 %0, %0, %18, %19\n\tv_min3_f32 %1, %1, %18, %19\n\tv_max3_f32 %2, %2, %20, %21\n\tv_min3_f32 %3, %3, %20, %21\n\t"
      "v_max3_f32 %0, %0, %22, %23\n\tv_min3_f32 %1, %1, %22, %23\n\tv_max_f32 %0, %0, %2\n\tv_min_f32 %1, %1, %3"
      : "=&v"(mx0), "=&v"(mn0), "=&v"(mx1), "=&v"(mn1)
      : "v"(pv[0].x), "v"(pv[0].y), "v"(pv[1].x), "v"(pv[1].y), "v"(pv[2].x), "v"(pv[2].y), "v"(pv[3].x), "v"(pv[3].y), "v"(pv[4].x), "v"(pv[4].y),
        "v"(pv[5].x), "v"(pv[5].y), "v"(pv[6].x), "v"(pv[6].y), "v"(pv[7].x), "v"(pv[7].y), "v"(pv[8].x), "v"(pv[8].y), "v"(pv[9].x), "v"(pv[9].y));
  const float mxl = -mx0 * LOG2E, mnl = mn0 * LOG2E;
  const f32x2 l2 = {LOG2E, LOG2E}, nl2 = {-LOG2E, -LOG2E}, mx2 = {mxl, mxl}, mn2 = {mnl, mnl};
  f32x2 ea[10], eb[10];
  hook(0); SCHED();
#pragma unroll
  for (int i = 0; i < 10; ++i) { ea[i] = __builtin_elementwise_fma(pv[i], l2, mx2); eb[i] = __builtin_elementwise_fma(pv[i], nl2, mn2); }
  hook(1); SCHED();
#pragma unroll
  for (int i = 0; i < 10; ++i) {
    ea[i].x = __builtin_amdgcn_exp2f(ea[i].x); ea[i].y = __builtin_amdgcn_exp2f(ea[i].y);
    eb[i].x = __builtin_amdgcn_exp2f(eb[i].x); eb[i].y = __builtin_amdgcn_exp2f(eb[i].y);
    if (i == 4) { hook(2); SCHED(); }
  }
  hook(3); SCHED();
  f32x2 sa0 = ea[0] + ea[1], sb0 = eb[0] + eb[1], sa1 = ea[2] + ea[3], sb1 = eb[2] + eb[3];
  SCHED();
#pragma unroll
  for (int i = 4; i < 10; i += 2) { sa0 += ea[i]; sb0 += eb[i]; sa1 += ea[i + 1]; sb1 += eb[i + 1]; SCHED(); }
  sa0 += sa1; sb0 += sb1;
  const float ra = __builtin_amdgcn_rcpf(sa0.x + sa0.y), rb = __builtin_amdgcn_rcpf(sb0.x + sb0.y);
  const f32x2 ra2 = {ra, ra}, rb2 = {rb, rb};
  hook(4); SCHED();
#pragma unroll
  for (int i = 0; i < 10; ++i) { ea[i] *= ra2; eb[i] *= rb2; }
  hook(5); SCHED();
#pragma unroll
  for (int i = 0; i < 10; ++i) { pos[i] = pkv(ea[i]); neg[i] = pkv(eb[i]); }
  SCHED();
}

DI void store_x(const f32x16& xr, const float (&xr4)[8], rsrc_t rsW, int l, int tt2, int nq, int w, int lane) {
  const int vo = XB_OFF + l * XB_SZ + tt2 * 40960 + nq * 10240;
  st128(v4u{pk2(xr[0], xr[1]), pk2(xr[2], xr[3]), pk2(xr[4], xr[5]), pk2(xr[6], xr[7])}, rsW, lane * 16, vo + 2 * w * 1024);
  st128(v4u{pk2(xr[8], xr[9]), pk2(xr[10], xr[11]), pk2(xr[12], xr[13]), pk2(xr[14], xr[15])}, rsW, lane * 16 + 1024, vo + 2 * w * 1024);
  f32x2 s0 = mk2(xr[0], xr[1]), q0 = s0 * s0, s1 = mk2(xr[2], xr[3]), q1 = s1 * s1;
  if (w < 2) {
    st128(v4u{pk2(xr4[0], xr4[1]), pk2(xr4[2], xr4[3]), pk2(xr4[4], xr4[5]), pk2(xr4[6], xr4[7])}, rsW, lane * 16, vo + (8 + w) * 1024);
#pragma unroll
    for (int i = 0; i < 4; i += 2) {
      const f32x2 v = mk2(xr4[2 * i], xr4[2 * i + 1]), u = mk2(xr4[2 * i + 2], xr4[2 * i + 3]);
      s0 += v; q0 = __builtin_elementwise_fma(v, v, q0); s1 += u; q1 = __builtin_elementwise_fma(u, u, q1);
    }
  }
#pragma unroll
  for (int i = 2; i < 8; i += 2) {
    const f32x2 v = mk2(xr[2 * i], xr[2 * i + 1]), u = mk2(xr[2 * i + 2], xr[2 * i + 3]);
    s0 += v; q0 = __builtin_elementwise_fma(v, v, q0); s1 += u; q1 = __builtin_elementwise_fma(u, u, q1);
  }
  s0 += s1; q0 += q1;
  const auto sw = __builtin_amdgcn_permlane32_swap(__float_as_uint(s0.x + s0.y), __float_as_uint(q0.x + q0.y), false, false);
  const float t = __uint_as_float(sw[0]) + __uint_as_float(sw[1]);
  __builtin_amdgcn_raw_buffer_store_b32(__float_as_uint(t), rsW, lane * 4, SB_OFF + l * SB_SZ + (tt2 * 16 + nq * 4 + w) * 256, AUX_SC1);
}

constexpr int XW0 = 6, XW = 16;

__global__ void __launch_bounds__(256, 1) main_kernel(Params p) {
  extern __shared__ __attribute__((aligned(16))) unsigned char smem[];
  LAS unsigned char* L = (LAS unsigned char*)smem;
  const int tid0 = threadIdx.x, w = __builtin_amdgcn_readfirstlane(tid0 >> 6);
  int mt, hp;
  block_map(blockIdx.x, mt, hp);
  unsigned char* ws = p.ws;
  const rsrc_t rsW = __builtin_amdgcn_make_buffer_rsrc((void*)ws, 0, WS_BYTES, 0x00020000);
  const unsigned ldsbase = (unsigned)(size_t)L;
  const int tt = 4 * mt + w;
  const int tt2 = 4 * mt + (hp >> 2), nq = hp & 3;
  f32x16 xr;
  float xr4[8];
  u64 tag;
  v4u xa, xb, xc;
  unsigned pref;
  {
    const int tid = tid0, lane = tid & 63, h = lane >> 5, r = lane & 31, cu = mt * 16 + hp;
    STAMP(0);
    {
      const int l60 = lane < 60 ? lane : 59, row = l60 / 15, c = l60 % 15;
      const unsigned vo = row * 3840 + (c / 5) * 1280 + (c % 5) * 16;
      const int so = (40 * mt + 4 * w) * 3840 + hp * 80;
      dma1((const unsigned char*)p.wA + so, vo, ldsbase + S_W + w * 1024);
      dma1((const unsigned char*)p.wA + so + 16 * 3840, vo, ldsbase + S_W + w * 1024 + 4096);
      if (w < 2) dma1((const unsigned char*)p.wA + so + 32 * 3840, vo, ldsbase + S_W + w * 1024 + 8192);
      const unsigned char* xs = (const unsigned char*)p.x0 + (32 * tt2 + r) * 1280 + h * 16 + nq * 320;
      xa = *(const v4u*)(xs + w * 64); xb = *(const v4u*)(xs + w * 64 + 32); xc = *(const v4u*)(xs + 256 + (w & 1) * 32);
      tag = __hip_atomic_load((gu64*)(ws + EP_OFF), RLX_AGENT);
      u64 pc;
      asm volatile("s_getpc_b64 %0" : "=s"(pc));
      asm volatile("global_load_dword %0, %1, %2" : "=v"(pref) : "v"((tid < CODE_LINES ? tid : 0) * 128), "s"(pc & ~127ull));
      SCHED();
      if (w < 3) {
        const int q = (lane >> 2) & 7;
        const int orow = w < 2 ? (2 * mt + h) * 20 + 8 * w + q : (2 * ((mt & ~1) + (q >> 2)) + h) * 20 + 16 + (q & 3);
        const unsigned vw = orow * 1280 + (lane & 3) * 16;
#pragma nounroll
        for (int n = 0; n < (hp < 4 ? 10 : 5); ++n) {
          const int tl = n < 5 ? n : n - 5, t = tl == 0 ? 4 : (((mt >> 2) + tl) & 3);
          dma1((const unsigned char*)(n < 5 ? p.woA : p.woB) + nq * 320 + t * 64, vw, ldsbase + S_WO + (w * 5 + tl) * 2048 + (n < 5 ? 0 : 1024));
        }
      } else {
        dma1((const unsigned char*)p.wphi + hp * 1600, lane * 16, ldsbase + S_PH);
        dma1((const unsigned char*)p.wphi + hp * 1600, (lane < 36 ? 64 + lane : 99) * 16, ldsbase + S_PH + 1024);
      }
      dma1((const unsigned char*)p.wB + so, vo, ldsbase + S_W + 10240 + w * 1024);
      dma1((const unsigned char*)p.wB + so + 16 * 3840, vo, ldsbase + S_W + 10240 + w * 1024 + 4096);
      if (w < 2) dma1((const unsigned char*)p.wB + so + 32 * 3840, vo, ldsbase + S_W + 10240 + w * 1024 + 8192);
    }
    if (tid < 3) __hip_atomic_store((gu32*)(ws + CT_OFF + (tid * 256 + cu) * 256), 0u, RLX_AGENT);
    if (tid < 5) sts128(L, L_ZERO + tid * 16, v4u{0u, 0u, 0u, 0u});
    int ja0, ja1, jvo, jvs;
    bool jst = true;
    {
      const int blk = (lane >> 4) & 1, q = (lane & 15) >> 2, pp = lane & 3;
      if (w < 2) {
        const int tile = 2 * w + h, c = 2 * blk + (pp >> 1), hh = pp & 1;
        const int st = tile < 3 ? tile : (c < 3 ? c : 2), sec = st == 2 ? 0 : st + 1, cc = tile < 3 ? c : 4;
        ja0 = S_W + q * 240 + sec * 80 + hh * 40 + cc * 8; ja1 = ja0 + 1024;
        jvs = tile < 3 ? 512 : 384;
        jvo = tile * 40960 + 5 * mt * jvs + r * 16;
        jst = tile < 3 || r < 24;
      } else {
        ja0 = S_WO + (w - 2) * 10240 + (8 * h + q) * 64 + blk * 32 + (pp & 1) * 16 + (pp >> 1) * 8; ja1 = ja0 + 256;
        jvs = 10240;
        jvo = lane * 16;
      }
    }
    STAMP(49);
    DRAIN();
    asm volatile("" :: "v"(pref));
    STAMP(50);
#pragma unroll
    for (int i = 0; i < 4; ++i) {
      xr[2 * i] = bflo(xa[i]); xr[2 * i + 1] = bfhi(xa[i]);
      xr[8 + 2 * i] = bflo(xb[i]); xr[8 + 2 * i + 1] = bfhi(xb[i]);
      xr4[2 * i] = bflo(xc[i]); xr4[2 * i + 1] = bfhi(xc[i]);
    }
    tag += 1ull;
    BAR();
#pragma nounroll
    for (int job = 0; job < 4; ++job) {
      const int ty = job >> 1, sec = job & 1;
      if ((!sec || w == 3) && (w < 2 || !ty || hp < 4)) {
        const int lo = w < 2 ? ty * 10240 : ty * 1024 + sec * 10240;
        const int u = sec ? 8 + ((mt & 3) >> 1) : 2 * (mt & 3) + (w - 2);
        const int so = w < 2 ? WT_OFF + (ty * 16 + hp) * WT_IMG : (ty ? WOB_OFF : WOA_OFF) + hp * 204800 + ((mt >> 2) * 50 + u) * 1024;
        v4u f[5];
#pragma unroll
        for (int n = 0; n < 5; ++n) f[n] = trfrag(L, ja0 + lo + n * 2048, ja1 + lo + n * 2048);
        SCHED();
        if (jst) {
#pragma unroll
          for (int n = 0; n < 5; ++n) st128(f[n], rsW, jvo + n * jvs, so);
        }
        if (w < 2) {
          f32x2 s0 = {0.f, 0.f}, s1 = {0.f, 0.f};
#pragma unroll
          for (int n = 0; n < 5; ++n) {
            s0 += mk2(bflo(f[n].x), bfhi(f[n].x)); s1 += mk2(bflo(f[n].y), bfhi(f[n].y));
            s0 += mk2(bflo(f[n].z), bfhi(f[n].z)); s1 += mk2(bflo(f[n].w), bfhi(f[n].w));
          }
          s0 += s1;
          __builtin_amdgcn_raw_buffer_store_b32(__float_as_uint(s0.x + s0.y), rsW, tid * 4, CS_OFF + ((ty * 16 + hp) * 16 + mt) * 512, AUX_SC1);
        }
      }
      if (job == 1) {
        store_x(xr, xr4, rsW, 0, tt2, nq, w, lane);
        STAMP(51);
        DRAIN();
        BAR();
        if (tid < 32) {
          const int dst = tid < 16 ? mt * 16 + tid : (tid - 16) * 16 + hp, slot = tid < 16 ? hp : 16 + mt;
          __hip_atomic_store((gu64*)(ws + TG_OFF + dst * 256 + slot * 8), tag, RLX_AGENT);
        }
        STAMP(61);
      }
    }
    if (w < 3) {
      const int c = r >> 3, hi = (r >> 2) & 1, e = r & 3, s = w;
#pragma unroll
      for (int tile = 0; tile < 2; ++tile) {
        const int f = tile == 0 ? 4 * c + e : 16 + e;
        const bool rowok = (hi == h) && (tile == 0 || c == 0);
        unsigned v[8];
#pragma unroll
        for (int j = 0; j < 8; ++j) {
          const unsigned t = *(LAS u16*)(L + S_PH + h * 800 + s * 320 + f * 2 + j * 40);
          v[j] = (rowok && (j < 4 || s < 2)) ? t : 0u;
        }
        sts128(L, L_WPH + (tile * 3 + s) * 1024 + lane * 16, v4u{v[0] | (v[1] << 16), v[2] | (v[3] << 16), v[4] | (v[5] << 16), v[6] | (v[7] << 16)});
      }
    }
    STAMP(52);
    if (w == 0) wait_tags(ws, TG_OFF + cu * 256 + r * 8, tag);
    BAR();
    STAMP(62);
    const unsigned char* w0 = ws + WT_OFF + hp * WT_IMG;
    dma_range(w0, ldsbase, 0, 40, w, 4, lane * 16);
    dma_range(w0, ldsbase, 60, 75, 3 - w, 4, lane * 16);
    if (w == 0) cs_build(rsW, L, 0, hp, lane);
    dma_range(w0, ldsbase, 40, 60, w, 4, lane * 16);
    asm volatile("s_waitcnt vmcnt(10)" ::: "memory");
    BAR();
  }

  for (int l = 0; l < NLAYERS; ++l) {
    int tid = tid0;
    OPAQUE(tid);
    const int lane = tid & 63, h = lane >> 5, r = lane & 31, lane16 = lane * 16;
    const bool isB = l & 1;
    const int xso = XB_OFF + l * XB_SZ + tt * 40960;
    const int sso = SB_OFF + l * SB_SZ + tt * 4096;
    const int wov = (isB ? WOB_OFF + nq * 204800 : WOA_OFF + (l == 0 ? hp : nq) * 204800) + w * 51200;
    int at0 = L_T3 + (h * 24 + (r < 24 ? r : 23)) * 16, am = L_WPH + lane16;
    OPAQUE(at0); OPAQUE(am);
    int rot = hp;
    asm volatile("" : "+s"(rot));
#define CH(ks) ((((ks) / 5) + rot) & 7)
#define AKV(ks) (lane16 + CH(ks) * 5120 + ((ks) % 5) * 1024)
#define AT3(ks) (at0 + CH(ks) * 3840 + ((ks) % 5) * 768)
    STAMP(1 + l * 12 + 0);
    v4u xf[40], wo[50];
#define LDX(ks) ld128(rsW, lane16 + (((ks) % 5) & 3) * 1024, xso + CH(ks) * 5120 + (((ks) % 5) >> 2) * 4096)
#define LDWO(i) wo[i] = ld128(rsW, lane16 + ((i) & 3) * 1024, wov + ((i) >> 2) * 4096)
#pragma unroll
    for (int ks = 0; ks < XW0; ++ks) { xf[ks] = LDX(ks); SCHED(); }
    f32x16 aK, aV, aQ, aT;
    float rs;
    v4u mb, csq;
    {
      v4u fk[3], fv[3], ft[3];
      v2u cs[4];
      float sp[16];
#pragma unroll
      for (int ks = 0; ks < 2; ++ks) { fk[ks] = lds128(L, AKV(ks)); ft[ks] = lds128(L, AT3(ks)); fv[ks] = lds128(L, AKV(ks) + L_V); }
      SCHED();
      float mean = 0.f, var = 0.f;
      sfor<0, 40>(LAMBDA(KS) {
        constexpr int ks = decltype(KS)::value;
        constexpr int c = ks % 3, n = (ks + 2) % 3;
        if constexpr (ks == 0) MFMA0B(aK, fk[c], xf[ks]); else MFMAB(aK, fk[c], xf[ks]);
        if constexpr (ks + 2 < 40) fk[n] = lds128(L, AKV(ks + 2));
        if constexpr (ks < XW - XW0) xf[XW0 + 2 * ks] = LDX(XW0 + 2 * ks);
        else if constexpr (ks + XW < 40) xf[ks + XW] = LDX(ks + XW);
        if constexpr (ks >= 26) LDWO(ks - 26);
        SCHED();
        if constexpr (ks == 0) MFMA0B(aT, ft[c], xf[ks]); else MFMAB(aT, ft[c], xf[ks]);
        if constexpr (ks + 2 < 40) ft[n] = lds128(L, AT3(ks + 2));
        if constexpr (ks >= 8 && ks < 24) sp[ks - 8] = __uint_as_float(__builtin_amdgcn_raw_buffer_load_b32(rsW, lane * 4 + (ks - 8) * 256, sso, AUX_SC1));
        if constexpr (ks == 30) {
#pragma unroll
          for (int i = 0; i < 4; ++i) cs[i] = *(LAS v2u*)(L + L_CS + (l & 1) * 1024 + i * 256 + r * 8);
        }
        if constexpr (ks == 33) { sp[0] += sp[1]; sp[2] += sp[3]; sp[4] += sp[5]; sp[6] += sp[7]; }
        if constexpr (ks == 34) { sp[0] += sp[2]; sp[4] += sp[6]; sp[8] += sp[10]; sp[12] += sp[14]; }
        if constexpr (ks == 35) {
          const auto sw = __builtin_amdgcn_permlane32_swap(__float_as_uint(sp[0]), __float_as_uint(sp[0]), false, false);
          mean = __uint_as_float(sw[0]) * (1.f / 640.f); var = __uint_as_float(sw[1]);
        }
        if constexpr (ks == 36) { const unsigned nm = h ? 0u : pk2(-mean, -mean); mb = v4u{nm, nm, 0u, 0u}; }
        if constexpr (ks == 37) { var = __builtin_amdgcn_sqrtf(var); var = rbf(var); }
        SCHED();
        if constexpr (ks == 0) MFMA0B(aV, fv[c], xf[ks]); else MFMAB(aV, fv[c], xf[ks]);
        if constexpr (ks + 2 < 40) fv[n] = lds128(L, AKV(ks + 2) + L_V);
        if constexpr (ks < XW - XW0) xf[XW0 + 2 * ks + 1] = LDX(XW0 + 2 * ks + 1);
        if constexpr (ks == 33) { sp[8] += sp[9]; sp[10] += sp[11]; sp[12] += sp[13]; sp[14] += sp[15]; }
        if constexpr (ks == 34) { sp[0] += sp[4]; sp[8] += sp[12]; sp[0] += sp[8]; }
        if constexpr (ks == 35) { var = fmaxf(fmaf(var, 1.f / 640.f, -mean * mean), 0.f); }
        if constexpr (ks == 36) { var = rbf(var) + 1e-5f; var = rbf(var); }
        if constexpr (ks == 37) { rs = __builtin_amdgcn_rcpf(var); }
        SCHED();
        if constexpr (ks == 38) {
          const v4u c0 = {cs[0].x, cs[0].y, 0u, 0u}, c1 = {cs[1].x, cs[1].y, 0u, 0u}, c3 = {cs[3].x, cs[3].y, 0u, 0u};
          csq = v4u{cs[2].x, cs[2].y, 0u, 0u};
          MFMAN(aK, c0, mb); MFMAN(aT, c3, mb); MFMAN(aV, c1, mb);
          SCHED();
        }
      });
    }
    STAMP(1 + l * 12 + 1);
    {
      unsigned pos[10];
      f32x2 a[10];
      const f32x2 rs2 = {rs, rs};
      const int row = h * 8192 + (32 * w + r) * 64, sw = (r >> 1) & 3;
      const int u0 = row + (0 ^ sw) * 16, u1 = row + (1 ^ sw) * 16, u2 = row + (2 ^ sw) * 16;
      v4u wf[6];
#pragma unroll
      for (int i = 0; i < 6; ++i) wf[i] = lds128(L, am + i * 1024);
      SCHED();
      asm volatile("s_nop 5" : "+v"(aK), "+v"(aT), "+v"(aV));
#pragma unroll
      for (int i = 0; i < 8; ++i) a[i] = mk2(aK[2 * i], aK[2 * i + 1]);
      a[8] = mk2(aT[0], aT[1]); a[9] = mk2(aT[2], aT[3]);
#define VPART() do { \
        f32x2 t[10]; \
        _Pragma("unroll") for (int i = 0; i < 8; ++i) t[i] = mk2(aV[2 * i], aV[2 * i + 1]) * rs2; \
        t[8] = mk2(aT[4], aT[5]) * rs2; t[9] = mk2(aT[6], aT[7]) * rs2; \
        LDWO(40); SCHED(); \
        unsigned vv[10]; \
        _Pragma("unroll") for (int i = 0; i < 10; ++i) vv[i] = pkv(t[i]); \
        LDWO(41); SCHED(); \
        BAR();     \
        sts128(L, L_IV + u0, v4u{vv[0], vv[1], vv[2], vv[3]}); \
        sts128(L, L_IV + u1, v4u{vv[4], vv[5], vv[6], vv[7]}); \
        sts128(L, L_IV + u2, v4u{vv[8], vv[9], 0x00003f80u, 0u}); \
      } while (0)
      auto hk = [&](int n) __attribute__((always_inline)) {
        if (n == 0) { LDWO(42); LDWO(43); } if (n == 1) { LDWO(44); LDWO(45); } if (n == 2) { LDWO(46); LDWO(47); }
        if (n == 3) { LDWO(48); } if (n == 4) { LDWO(49); }
      };
      if (!isB) {
        VPART();
        phiA(a, rs, pos, hk);
      } else {
        f32x16 d0, d1;
        unsigned neg[10];
        phiB_mm(wf, a, rs, d0, d1);
        VPART();
        OPAQUE(d0); OPAQUE(d1);
        phiB_sm(d0, d1, pos, neg, hk);
        sts128(L, L_IPK1 + u0, v4u{neg[0], neg[1], neg[2], neg[3]});
        sts128(L, L_IPK1 + u1, v4u{neg[4], neg[5], neg[6], neg[7]});
        sts128(L, L_IPK1 + u2, v4u{neg[8], neg[9], 0u, 0u});
      }
      sts128(L, L_IPK0 + u0, v4u{pos[0], pos[1], pos[2], pos[3]});
      sts128(L, L_IPK0 + u1, v4u{pos[4], pos[5], pos[6], pos[7]});
      sts128(L, L_IPK0 + u2, v4u{pos[8], pos[9], 0u, 0u});
      BAR();
    }
    STAMP(1 + l * 12 + 2);
    const int nroles = isB ? 4 : 2;
    const int kvp = KVP_OFF + l * KVP_SZ + hp * 16 * KVP_ONE;
    if (w < nroles) {
      const int head = w & 1, part = w >> 1;
      const int g = lane >> 4, kh = g >> 1, blk = g & 1, q = (lane & 15) >> 2, pp = lane & 3;
      const int un = 2 * blk + (pp >> 1);
      const int a0 = head * 8192 + (8 * kh + q) * 64 + ((un ^ (q >> 1)) * 16) + 8 * (pp & 1);
      const int a1 = head * 8192 + (8 * kh + 4 + q) * 64 + ((un ^ (2 + (q >> 1))) * 16) + 8 * (pp & 1);
      const int pb = part * 16384;
      v4u fa[8], fb[8];
#pragma unroll
      for (int s = 0; s < 8; ++s) {
        fa[s] = trfrag(L, pb + a0 + s * 1024, pb + a1 + s * 1024);
        fb[s] = trfrag(L, L_IV + a0 + s * 1024, L_IV + a1 + s * 1024);
        SCHED();
      }
      f32x16 d;
      MFMA0(d, fa[0], fb[0]);
#pragma unroll
      for (int s = 1; s < 8; ++s) MFMA(d, fa[s], fb[s]);
      MFENCE1(d);
      if (r < 21) {
        const v4u ua = {pk2(d[0], d[1]), pk2(d[2], d[3]), pk2(d[4], d[5]), pk2(d[6], d[7])};
        st128(ua, rsW, ((w * 2 + h) * 21 + r) * 16, kvp + mt * KVP_ONE);
        if (h == 0) {
          const v2u ub = {pk2(d[8], d[9]), pk2(d[10], d[11])};
          __builtin_amdgcn_raw_buffer_store_b64(ub, rsW, nroles * 672 + (w * 21 + r) * 8, kvp + mt * KVP_ONE, AUX_SC1);
        }
      }
    }
    {
      constexpr int QD = 4, Q1 = 16;
      int aq0 = L_Q + lane16;
      OPAQUE(aq0);
#define AQ(ks) (aq0 + CH(ks) * 5120 + ((ks) % 5) * 1024)
      v4u fq[QD + 1];
#pragma unroll
      for (int ks = 0; ks < QD; ++ks) fq[ks] = lds128(L, AQ(ks));
      SCHED();
      asm volatile("s_nop 3");
      MFMA0(aQ, csq, mb);
#pragma unroll
      for (int ks = 0; ks < 40; ++ks) {
        MFMAB(aQ, fq[ks % (QD + 1)], xf[ks]);
        if (ks + QD < 40) fq[(ks + QD) % (QD + 1)] = lds128(L, AQ(ks + QD));
        if (ks >= Q1 && ((ks - Q1) % 2) == 0) LDWO(14 + (ks - Q1) / 2);
        SCHED();
        if (ks == Q1 - 1) {
          DRAIN();
          __builtin_amdgcn_s_barrier();
          publish(ws, 1, mt, hp, tid);
          STAMP(1 + l * 12 + 3);
          asm volatile("s_nop 3");
          SCHED();
        }
      }
    }
    const int nA = 42 * nroles, nU = nA + (21 * nroles + 1) / 2;
    int d0a, d1a;
    if (tid < nA) {
      const int role = tid / 42, rem = tid % 42, hh = rem / 21, e = rem % 21;
      d0a = L_KVI + (role & 1) * KVI_HS + e * 96 + (role >> 1) * 40 + hh * 8;
      d1a = d0a + 16;
    } else {
      const int c0 = 2 * (tid - nA), c1 = c0 + 1;
      const int r0 = c0 / 21, e0 = c0 % 21, r1 = c1 / 21, e1 = c1 % 21;
      d0a = L_KVI + (r0 & 1) * KVI_HS + e0 * 96 + (r0 >> 1) * 40 + 32;
      d1a = L_KVI + (r1 & 1) * KVI_HS + e1 * 96 + (r1 >> 1) * 40 + 32;
    }
    int b0, b1;
    {
      const int c = r >> 3, hi = (r >> 2) & 1, e = r & 3;
      b0 = hi == h ? L_KVI + h * KVI_HS + (4 * c + e) * 96 : L_ZERO;
      b1 = hi == h ? L_KVI + h * KVI_HS + (c == 0 ? 16 + e : 20) * 96 : L_ZERO;
    }
    OPAQUE(d0a); OPAQUE(d1a); OPAQUE(b0); OPAQUE(b1);
    v4u sel;
    {
      const bool on = ((r >> 2) & 1) == h && r < 16;
      const int j = (r >> 3) * 4 + (r & 3);
      const unsigned one = on ? ((j & 1) ? 0x3f800000u : 0x00003f80u) : 0u;
      sel = v4u{(j >> 1) == 0 ? one : 0u, (j >> 1) == 1 ? one : 0u, (j >> 1) == 2 ? one : 0u, (j >> 1) == 3 ? one : 0u};
    }
    STAMP(1 + l * 12 + 4);
    unsigned pq[20];
    v4u pp[16];
    {
      f32x2 a[10];
      unsigned pos[10], neg[10], seen;
      v4u wf[6];
#pragma unroll
      for (int i = 0; i < 6; ++i) wf[i] = lds128(L, am + i * 1024);
      SCHED();
      asm volatile("s_nop 5" : "+v"(aQ));
#pragma unroll
      for (int i = 0; i < 8; ++i) a[i] = mk2(aQ[2 * i], aQ[2 * i + 1]);
      a[8] = mk2(aT[8], aT[9]); a[9] = mk2(aT[10], aT[11]);
      const int pv = (tid < nU ? tid : nU - 1) * 16;
      auto fetch = [&]() __attribute__((always_inline)) {
        SCHED();
        wait_ctr(ws, 1, mt, hp, 16u * (l + 1), seen);
        STAMP(1 + l * 12 + 5);
#pragma unroll
        for (int m = 0; m < 16; ++m) { pp[m] = ld128(rsW, pv, kvp + m * KVP_ONE); SCHED(); }
      };
      if (!isB) {
        phiA(a, rs, pos, [&](int n) __attribute__((always_inline)) { if (n == 1) seen = poll_ctr(ws, 1, mt, hp); if (n == 5) fetch(); });
#pragma unroll
        for (int i = 0; i < 10; ++i) neg[i] = 0u;
      } else {
        f32x16 d0, d1;
        phiB_mm(wf, a, rs, d0, d1);
        seen = poll_ctr(ws, 1, mt, hp);
        MFENCE2(d0, d1);
        phiB_sm(d0, d1, pos, neg, [&](int n) __attribute__((always_inline)) { if (n == 2) fetch(); });
      }
#pragma unroll
      for (int i = 0; i < 10; ++i) { pq[i] = pos[i]; pq[10 + i] = neg[i]; }
    }
    {
      f32x16 d;
      MFMA0N(d, sel, pp[0]);
#pragma unroll
      for (int m = 1; m < 16; ++m) MFMA(d, sel, pp[m]);
      MFENCE1(d);
      if (tid < nU) {
        *(LAS v2u*)(L + d0a) = v2u{pk2(d[0], d[1]), pk2(d[2], d[3])};
        *(LAS v2u*)(L + d1a) = v2u{pk2(d[4], d[5]), pk2(d[6], d[7])};
      }
    }
    BAR();
    STAMP(1 + l * 12 + 6);
    {
      v4u fa[5], fb[5];
#pragma unroll
      for (int s = 0; s < 5; ++s) { fb[s] = lds128(L, b1 + 16 * s); fa[s] = lds128(L, b0 + 16 * s); SCHED(); }
      f32x16 n0, n1;
      const v4u p0 = {pq[0], pq[1], pq[2], pq[3]}, p1 = {pq[4], pq[5], pq[6], pq[7]}, p2 = {pq[8], pq[9], pq[10], pq[11]};
      const v4u p3 = {pq[12], pq[13], pq[14], pq[15]}, p4 = {pq[16], pq[17], pq[18], pq[19]};
      MFMA0N(n1, fb[0], p0); MFMA0(n0, fa[0], p0);
      MFMA(n1, fb[1], p1); MFMA(n0, fa[1], p1);
      if (!isB) { fa[2].z = 0u; fa[2].w = 0u; fb[2].z = 0u; fb[2].w = 0u; }
      MFMAN(n1, fb[2], p2); MFMA(n0, fa[2], p2);
      if (isB) { MFMAN(n1, fb[3], p3); MFMA(n0, fa[3], p3); MFMA(n1, fb[4], p4); MFMA(n0, fa[4], p4); }
      MFENCE2(n0, n1);
      const float inv = __builtin_amdgcn_rcpf(n1[4] + 1e-6f);
      const f32x2 iv = {inv, inv};
      f32x2 t[10];
#pragma unroll
      for (int i = 0; i < 8; ++i) t[i] = mk2(n0[2 * i], n0[2 * i + 1]) * iv;
      t[8] = mk2(n1[0], n1[1]) * iv; t[9] = mk2(n1[2], n1[3]) * iv;
      SCHED();
      const v4u o0 = {pkv(t[0]), pkv(t[1]), pkv(t[2]), pkv(t[3])};
      const v4u o1 = {pkv(t[4]), pkv(t[5]), pkv(t[6]), pkv(t[7])};
      const v2u o2 = {pkv(t[8]), pkv(t[9])};
      const int ob = OB_OFF + l * XB_SZ + (tt * 16 + hp) * 2560;
      st128(o0, rsW, lane16, ob);
      st128(o1, rsW, lane16 + 1024, ob);
      __builtin_amdgcn_raw_buffer_store_b64(o2, rsW, lane * 8 + 2048, ob, AUX_SC1);
    }
    DRAIN();
    BAR();
    publish(ws, 2, mt, hp, tid);
    STAMP(1 + l * 12 + 7);
    const unsigned char* wnext = ws + WT_OFF + (((l + 1) & 1) * 16 + hp) * WT_IMG;
    if (l == 0 && w == 3) cs_build(rsW, L, 1, hp, lane);
    const bool more = l + 1 < NLAYERS && !NODMA;
#pragma unroll
    for (int i = 26; i < 40; ++i) { LDWO(i); SCHED(); }
    int rw0 = L_RED4 + w * 4096 + lane16, rw1 = L_RED + (((w + 1) & 3) * 3 + 2) * 4096 + lane16;
    int rw2 = L_RED + (((w + 2) & 3) * 3 + 1) * 4096 + lane16, rw3 = L_RED + (((w + 3) & 3) * 3) * 4096 + lane16;
    int rr0 = L_RED + w * 12288 + lane16, rr4 = L_RED4 + (w & 1) * 2048 + lane16;
    OPAQUE(rw0); OPAQUE(rw1); OPAQUE(rw2); OPAQUE(rw3); OPAQUE(rr0); OPAQUE(rr4);
    wait_ctr(ws, 2, mt, hp, 16u * (l + 1), 0u);
    STAMP(1 + l * 12 + 8);
    {
      v4u of[10];
      const int ob = OB_OFF + l * XB_SZ + (tt2 * 16 + 4 * w) * 2560;
      v2u oh[4];
#pragma unroll
      for (int a = 0; a < 4; ++a) {
        of[2 * a] = ld128(rsW, lane16, ob + a * 2560); SCHED();
        of[2 * a + 1] = ld128(rsW, lane16 + 1024, ob + a * 2560); SCHED();
      }
#pragma unroll
      for (int a = 0; a < 4; ++a) oh[a] = __builtin_amdgcn_raw_buffer_load_b64(rsW, lane * 8 + 2048, ob + a * 2560, AUX_SC1);
      SCHED();
      of[8] = v4u{oh[0].x, oh[0].y, oh[1].x, oh[1].y};
      of[9] = v4u{oh[2].x, oh[2].y, oh[3].x, oh[3].y};
      f32x16 acc[4];
      v4u rd[12], r4[8];
#pragma unroll
      for (int u = 0; u < 10; ++u) {
        if (u == 0) { MFMA0A(acc[0], wo[0], of[0]); MFMA0A(acc[1], wo[10], of[0]); }
        else {
          if (u == 8) asm volatile("s_nop 1" : "+v"(of[8]), "+v"(of[9]));
          MFMAA(acc[0], wo[u], of[u]); MFMAA(acc[1], wo[10 + u], of[u]);
        }
        if (u >= 8 && more) dma2k(wnext + ((u - 8) * 4 + w) * 2048, ldsbase + ((u - 8) * 4 + w) * 2048, lane16);
      }
#pragma unroll
      for (int tl = 2; tl < 5; ++tl) {
#pragma unroll
        for (int u = 0; u < 10; ++u) {
          if (tl < 4) { if (u == 0) MFMA0A(acc[tl], wo[tl * 10], of[0]); else MFMAA(acc[tl], wo[tl * 10 + u], of[u]); }
          else MFMA(xr, wo[tl * 10 + u], of[u]);
          const int st = tl == 2 ? (u < 6 ? 0 : 1) : tl - 1;
          const int q0 = tl == 2 ? (u < 6 ? u - 2 : u - 6) : (tl == 3 ? u - 2 : 2 * (u - 2));
          const int qn = tl == 4 ? 2 : 1;
#pragma unroll
          for (int qd = q0; qd < q0 + qn; ++qd)
            if (qd >= 0 && qd < 4) {
              if (qd == 0) OPAQUE(acc[st]);
              const int dst = st == 0 ? rw0 : (st == 1 ? rw1 : (st == 2 ? rw2 : rw3));
              sts128(L, dst + qd * 1024, __builtin_bit_cast(v4u, f32x4{acc[st][4 * qd], acc[st][4 * qd + 1], acc[st][4 * qd + 2], acc[st][4 * qd + 3]}));
            }
          if (tl < 4 && (u & 1) == 1 && u < 8 && more) dma2k(wnext + ((2 + (tl - 2) * 4 + (u >> 1)) * 4 + w) * 2048, ldsbase + ((2 + (tl - 2) * 4 + (u >> 1)) * 4 + w) * 2048, lane16);
          if (tl == 4 && u == 5) BAR();
          if (tl == 4 && u >= 5 && u < 9) {
#pragma unroll
            for (int i = 0; i < 3; ++i) rd[(u - 5) * 3 + i] = lds128(L, rr0 + (i * 4 + (u - 5)) * 1024);
#pragma unroll
            for (int i = 0; i < 2; ++i) r4[(u - 5) * 2 + i] = lds128(L, rr4 + (u - 5) * 4096 + i * 1024);
          }
          SCHED();
        }
      }
      STAMP(1 + l * 12 + 9);
      f32x4 sm[4];
#pragma unroll
      for (int qd = 0; qd < 4; ++qd) sm[qd] = __builtin_bit_cast(f32x4, rd[qd * 3]) + __builtin_bit_cast(f32x4, rd[qd * 3 + 1]);
      SCHED();
#pragma unroll
      for (int qd = 0; qd < 4; ++qd) sm[qd] += __builtin_bit_cast(f32x4, rd[qd * 3 + 2]);
      SCHED();
      OPAQUE(xr);
#pragma unroll
      for (int i = 0; i < 16; ++i) xr[i] += sm[i >> 2][i & 3];
      {
        const f32x4 t0 = (__builtin_bit_cast(f32x4, r4[0]) + __builtin_bit_cast(f32x4, r4[2])) + (__builtin_bit_cast(f32x4, r4[4]) + __builtin_bit_cast(f32x4, r4[6]));
        const f32x4 t1 = (__builtin_bit_cast(f32x4, r4[1]) + __builtin_bit_cast(f32x4, r4[3])) + (__builtin_bit_cast(f32x4, r4[5]) + __builtin_bit_cast(f32x4, r4[7]));
#pragma unroll
        for (int i = 0; i < 4; ++i) { xr4[i] += t0[i]; xr4[4 + i] += t1[i]; }
      }
    }
    if (l + 1 < NLAYERS) {
      store_x(xr, xr4, rsW, l + 1, tt2, nq, w, lane);
      DRAIN();
      BAR();
      publish(ws, 0, mt, hp, tid);
      STAMP(1 + l * 12 + 10);
      if (w > 0 && !NODMA) {
        dma_range(wnext, ldsbase, 60, 75, w - 1, 3, lane16);
        dma_range(wnext, ldsbase, 39, 60, w - 1, 3, lane16);
        asm volatile("s_waitcnt vmcnt(14)" ::: "memory");
      }
      if (w == 0) wait_ctr(ws, 0, mt, hp, 16u * (l + 1), 0u);
      BAR();
    }
  }
  {
    const int tid = tid0, lane = tid & 63, h = lane >> 5, r = lane & 31;
    unsigned char* o = (unsigned char*)p.out + (32 * tt2 + r) * 1280 + h * 16 + nq * 320;
    *(v4u*)(o + w * 64) = v4u{pk2(xr[0], xr[1]), pk2(xr[2], xr[3]), pk2(xr[4], xr[5]), pk2(xr[6], xr[7])};
    *(v4u*)(o + w * 64 + 32) = v4u{pk2(xr[8], xr[9]), pk2(xr[10], xr[11]), pk2(xr[12], xr[13]), pk2(xr[14], xr[15])};
    if (w < 2) *(v4u*)(o + 256 + w * 32) = v4u{pk2(xr4[0], xr4[1]), pk2(xr4[2], xr4[3]), pk2(xr4[4], xr4[5]), pk2(xr4[6], xr4[7])};
    if (blockIdx.x == 0 && tid == 0) __hip_atomic_store((gu64*)(ws + EP_OFF), tag, RLX_AGENT);
    STAMP(60);
  }
}

extern "C" void kernel_launch(void* const* d_in, const int* in_sizes, int n_in, void* d_out, int out_size, void* d_ws, size_t ws_size,
                              hipStream_t stream) {
  Params p;
  p.x0 = (const u16*)d_in[0]; p.wA = (const u16*)d_in[1]; p.woA = (const u16*)d_in[2];
  p.wB = (const u16*)d_in[4]; p.wphi = (const u16*)d_in[5]; p.woB = (const u16*)d_in[6];
  p.out = (u16*)d_out; p.ws = (unsigned char*)d_ws;
  (void)hipFuncSetAttribute((const void*)main_kernel, hipFuncAttributeMaxDynamicSharedMemorySize, L_BYTES);
  main_kernel<<<256, 256, L_BYTES, stream>>>(p);
}
```

```cpp
#include <hip/hip_runtime.h>
#include <stdint.h>

#define GAS __attribute__((address_space(1)))
#define LAS __attribute__((address_space(3)))
#define DI __device__ __forceinline__
#define RLX_AGENT __ATOMIC_RELAXED, __HIP_MEMORY_SCOPE_AGENT
typedef unsigned short u16;
typedef unsigned long long u64;
typedef GAS unsigned gu32;
typedef GAS u64 gu64;
typedef unsigned v4u __attribute__((ext_vector_type(4)));
typedef unsigned v2u __attribute__((ext_vector_type(2)));
typedef short s16x4 __attribute__((ext_vector_type(4)));
typedef __bf16 bf16x2 __attribute__((ext_vector_type(2)));
typedef float f32x2 __attribute__((ext_vector_type(2)));
typedef float f32x4 __attribute__((ext_vector_type(4)));
typedef float f32x16 __attribute__((ext_vector_type(16)));
typedef __amdgpu_buffer_rsrc_t rsrc_t;


constexpr int EP_OFF = 0;
constexpr int TG_OFF = 256;
constexpr int CT_OFF = TG_OFF + 256 * 1024;
constexpr int CS_OFF = CT_OFF + 3 * 256 * 256;
constexpr int SB_OFF = CS_OFF + 32 * 8192;
constexpr int SB_SZ = 64 * 16 * 256;
constexpr int KVP_OFF = SB_OFF + 4 * SB_SZ;
constexpr int KVP_ONE = 3840;
constexpr int KVP_SZ = 256 * KVP_ONE;
constexpr int XB_OFF = KVP_OFF + 4 * KVP_SZ;
constexpr int XB_SZ = 64 * 40960;
constexpr int OB_OFF = XB_OFF + 4 * XB_SZ;
constexpr int WT_OFF = OB_OFF + 4 * XB_SZ;
constexpr int WT_IMG = 153600;
constexpr int WO_OFF = WT_OFF + 32 * WT_IMG;
constexpr int WS_BYTES = WO_OFF + 8 * 204800;
constexpr int CODE_LINES = 256;

constexpr int L_K = 0, L_T3 = 40960, L_V = 71680, L_Q = 112640;
constexpr int L_WPH = 153600;
constexpr int L_CS = L_WPH + 6144;
constexpr int L_ZERO = L_CS + 2048;
constexpr int L_DUMMY = L_ZERO + 128;
constexpr int L_BYTES = 163840;
constexpr int L_IPK = 0, L_IV = 32768;
constexpr int L_KVI = 49152;
constexpr int KVI_HS = 24 * 96;
constexpr int L_RED = 88064;
constexpr int L_RED4 = L_RED + 49152;
constexpr int S_X = 0, S_PH = 40960, S_EP = 43008, S_JUNK = 44032, S_ST = 45056, S_W = L_Q, S_TY = 16384;
constexpr int OOB = 0x7ffffff0;

constexpr int AUX_SC1 = 16;
constexpr float LOG2E = 1.4426950408889634f;
constexpr unsigned SPIN_CAP = 1u << 21;

struct Params {
  const u16* x0; const u16* wA; const u16* woA; const u16* wB; const u16* wphi; const u16* woB;
  u16* out; unsigned char* ws;
};

DI f32x2 mk2(float a, float b) { return f32x2{a, b}; }
DI unsigned pkv(f32x2 v) { return __builtin_bit_cast(unsigned, __builtin_convertvector(v, bf16x2)); }
DI unsigned pk2(float a, float b) { return pkv(mk2(a, b)); }
DI float rbf(float a) { return __uint_as_float(pk2(a, a) << 16); }
DI float bflo(unsigned d) { return __uint_as_float(d << 16); }
DI float bfhi(unsigned d) { return __uint_as_float(d & 0xffff0000u); }
DI float exp2c(float x) { return __builtin_amdgcn_fmed3f(__builtin_amdgcn_exp2f(x), 0.f, 1.f); }
DI float dot2(unsigned a, unsigned b, float c) { return __builtin_amdgcn_fdot2_f32_bf16(__builtin_bit_cast(bf16x2, a), __builtin_bit_cast(bf16x2, b), c, false); }
DI f32x4 asf4(v4u v) { return __builtin_bit_cast(f32x4, v); }

#define MF "v_mfma_f32_32x32x16_bf16 "
#define MFMA0(acc, a, b) asm volatile(MF "%0, %1, %2, 0" : "=&v"(acc) : "v"(a), "v"(b))
#define MFMA(acc, a, b) asm volatile(MF "%0, %1, %2, %0" : "+v"(acc) : "v"(a), "v"(b))
#define MFMA0N(acc, a, b) asm volatile("s_nop 1\n\t" MF "%0, %1, %2, 0" : "=&v"(acc) : "v"(a), "v"(b))
#define MFMAN(acc, a, b) asm volatile("s_nop 1\n\t" MF "%0, %1, %2, %0" : "+v"(acc) : "v"(a), "v"(b))
#define MFMA0A(acc, a, b) asm volatile(MF "%0, %1, %2, 0" : "=&v"(acc) : "a"(a), "v"(b))
#define MFMAA(acc, a, b) asm volatile(MF "%0, %1, %2, %0" : "+v"(acc) : "a"(a), "v"(b))
#define MFMA0B(acc, a, b) asm volatile(MF "%0, %1, %2, 0" : "=&v"(acc) : "v"(a), "a"(b))
#define MFMAB(acc, a, b) asm volatile(MF "%0, %1, %2, %0" : "+v"(acc) : "v"(a), "a"(b))
#define MFENCE1(a) asm volatile("s_nop 7\n\ts_nop 3" : "+v"(a))
#define MFENCE2(a, b) asm volatile("s_nop 7\n\ts_nop 3" : "+v"(a), "+v"(b))
#define OPAQUE(x) asm volatile("" : "+v"(x))
#define DRAIN() asm volatile("s_waitcnt vmcnt(0)" ::: "memory")
#define VMWAIT(n) asm volatile("s_waitcnt vmcnt(" #n ")" ::: "memory")
#define SCHED() __builtin_amdgcn_sched_barrier(0)
#define BAR() do { asm volatile("s_waitcnt lgkmcnt(0)" ::: "memory"); __builtin_amdgcn_s_barrier(); asm volatile("" ::: "memory"); } while (0)
#define BAR0() do { asm volatile("" ::: "memory"); __builtin_amdgcn_s_barrier(); asm volatile("" ::: "memory"); } while (0)

template <int V> struct IC { static constexpr int value = V; };
template <int I, int N, class F> DI void sfor(F&& f) { if constexpr (I < N) { f(IC<I>{}); sfor<I + 1, N>(f); } }
#define LAMBDA(x) [&](auto x) __attribute__((always_inline))
#define LP(T, a) ((LAS T*)(size_t)(unsigned)(a))
DI v4u lds128(int a) { return *LP(v4u, a); }
DI void sts128(int a, v4u v) { *LP(v4u, a) = v; }
DI v4u trfrag(int a0, int a1) {
  s16x4 lo = __builtin_amdgcn_ds_read_tr16_b64_v4i16(LP(s16x4, a0));
  s16x4 hi = __builtin_amdgcn_ds_read_tr16_b64_v4i16(LP(s16x4, a1));
  v2u l = __builtin_bit_cast(v2u, lo), h = __builtin_bit_cast(v2u, hi);
  return v4u{l.x, l.y, h.x, h.y};
}
DI void st128(v4u v, rsrc_t rs, int vo, int so) {
  __builtin_amdgcn_raw_buffer_store_b128(v, rs, vo, so, AUX_SC1);
  asm volatile("s_nop 1" :: "v"(v) : "memory");
}
DI v4u ld128(rsrc_t rs, int vo, int so) { return __builtin_amdgcn_raw_buffer_load_b128(rs, vo, so, AUX_SC1); }

DI void block_map(int b, int& mt, int& hp) {
  const int x = b & 7, i = b >> 3;
  mt = 4 * (x >> 1) + (i & 3);
  hp = 8 * (x & 1) + (i >> 2);
}

DI void dma1(const void* gbase, unsigned voff, unsigned ldsoff) {
  asm volatile("s_mov_b32 m0, %1\n\ts_nop 3\n\tglobal_load_lds_dwordx4 %0, %2 sc1" :: "v"(voff), "s"(ldsoff), "s"(gbase) : "memory");
}
#define DMAK_(C, rs, lane16, so, lo, off) do { unsigned t_; \
  asm volatile("s_add_u32 m0, %3, %5\n\ts_add_u32 %0, %4, %5\n\tbuffer_load_dwordx4 %1, %2, %0 offen sc1 lds" \
               : "=&s"(t_) : "v"(lane16), "s"(rs), "s"(lo), "s"(so), C(off) : "memory", "scc"); } while (0)
#define DMAK(rs, lane16, so, lo, off) DMAK_("i", rs, lane16, so, lo, off)
#define DMAKS(rs, lane16, so, lo, off) DMAK_("s", rs, lane16, so, lo, off)

DI void cs_finish(int type, int lane, int src) {
  f32x2 v[16], s = {0.f, 0.f};
#pragma unroll
  for (int j = 0; j < 16; ++j) v[j] = *LP(f32x2, src + lane * 8 + j * 512);
  SCHED();
#pragma unroll
  for (int j = 0; j < 16; ++j) s += v[j];
  const float h0 = rbf(s.x), r0 = s.x - h0, m0 = rbf(r0), h1 = rbf(s.y), r1 = s.y - h1, m1 = rbf(r1);
  sts128(L_CS + type * 1024 + lane * 16, v4u{pk2(h0, m0), pk2(r0 - m0, 0.f), pk2(h1, m1), pk2(r1 - m1, 0.f)});
}
DI void wait_tags(unsigned char* ws, int off0, int off1, u64 tag) {
  gu64* f0 = (gu64*)(ws + off0);
  gu64* f1 = (gu64*)(ws + off1);
#pragma nounroll
  for (unsigned sp = 0; sp < SPIN_CAP; ++sp) {
    const u64 a = __hip_atomic_load(f0, RLX_AGENT), b = __hip_atomic_load(f1, RLX_AGENT);
    if (__all(a == tag && b == tag)) break;
  }
  __builtin_amdgcn_fence(__ATOMIC_ACQUIRE, "wavefront");
}
DI unsigned poll_ctr(unsigned char* ws, int type, int cu) {
  return __hip_atomic_load((gu32*)(ws + CT_OFF + (type * 256 + cu) * 256), RLX_AGENT);
}
DI void wait_ctr(unsigned char* ws, int type, int cu, unsigned val, unsigned seen, unsigned& budget) {
#pragma nounroll
  for (; budget != 0u && __builtin_amdgcn_readfirstlane(seen) < val; --budget) seen = poll_ctr(ws, type, cu);
  __builtin_amdgcn_fence(__ATOMIC_ACQUIRE, "wavefront");
}

DI void stat_acc(f32x2& s, f32x2& q, f32x2 v) { s += v; q = __builtin_elementwise_fma(v, v, q); }

constexpr int N0 = 10, S0 = 30;
constexpr int N2A = 8;
constexpr int N2B = 10;
constexpr int N3 = 2;
constexpr int N3B = 8;
constexpr int N4 = 4;
constexpr int N5 = 8;
constexpr int W1 = N0, W2 = W1 + N2A, W3 = W2 + N2B, W3B = W3 + N3, W4 = W3B + N3B, W5 = W4 + N4, W6 = W5 + N5;
static_assert(W6 == 50, "all wo fragments");
constexpr int QT = 15;
constexpr int D0 = 1, D1 = 16, DE = 48;
constexpr int D2 = 4;
constexpr int QX = 0;
constexpr int NV0 = 30;
constexpr int XW0 = 10;
constexpr int SP0 = 8;
constexpr int RLAG = 12;
#define VMWAITN(n) asm volatile("s_waitcnt vmcnt(%0)" :: "i"(n) : "memory")

__global__ void __launch_bounds__(256, 1) main_kernel(Params p) {
  const int tid = threadIdx.x, w = __builtin_amdgcn_readfirstlane(tid >> 6);
  const int lane = tid & 63, h = lane >> 5, r = lane & 31;
  int mt, hp;
  block_map(blockIdx.x, mt, hp);
  const int cu = mt * 16 + hp;
  unsigned char* ws = p.ws;
  const rsrc_t rsW = __builtin_amdgcn_make_buffer_rsrc((void*)ws, 0, WS_BYTES, 0x00020000);
  const unsigned ldsw = w * 1024;
  const int tt = 4 * mt + w;
  const int tt2 = 4 * mt + (hp >> 2), nq = hp & 3;
  f32x16 xr;
  f32x2 xr4[4];
  u64 tag;
  unsigned budget = SPIN_CAP;
  int lane16, lane8, lane4, at0, avq, u0, u1, u2, tr0, tr1, tv0, tv1, ksa, ksb, rdA0, rdA1, rdB0, rdB1, pvA, pvB, ob0, ob1, rw0, rw1, rw2, rw3, rr0, rr4, csa,
      x4o;
  GAS unsigned* pub_row; GAS unsigned* pub_col;
  v4u sel;

  auto store_x4 = [&](rsrc_t rs, int l, f32x2& s, f32x2& q) __attribute__((always_inline)) {
    st128(v4u{pkv(xr4[0]), pkv(xr4[1]), pkv(xr4[2]), pkv(xr4[3])}, rs, x4o, XB_OFF + l * XB_SZ + tt2 * 40960 + nq * 10240 + (8 + (w & 1)) * 1024);
    s = xr4[0]; q = s * s;
    stat_acc(s, q, xr4[1]); stat_acc(s, q, xr4[2]); stat_acc(s, q, xr4[3]);
    const float m = w < 2 ? 1.f : 0.f;
    s *= mk2(m, m); q *= mk2(m, m);
  };
  auto store_x = [&](int l, f32x2 s0, f32x2 q0) __attribute__((always_inline)) {
    const int vo = XB_OFF + l * XB_SZ + tt2 * 40960 + nq * 10240 + 2 * w * 1024;
    st128(v4u{pk2(xr[0], xr[1]), pk2(xr[2], xr[3]), pk2(xr[4], xr[5]), pk2(xr[6], xr[7])}, rsW, lane16, vo);
    st128(v4u{pk2(xr[8], xr[9]), pk2(xr[10], xr[11]), pk2(xr[12], xr[13]), pk2(xr[14], xr[15])}, rsW, lane16 + 1024, vo);
    f32x2 s1 = mk2(xr[0], xr[1]), q1 = s1 * s1;
    stat_acc(s0, q0, mk2(xr[2], xr[3]));
#pragma unroll
    for (int i = 2; i < 8; i += 2) { stat_acc(s1, q1, mk2(xr[2 * i], xr[2 * i + 1])); stat_acc(s0, q0, mk2(xr[2 * i + 2], xr[2 * i + 3])); }
    s0 += s1; q0 += q1;
    const auto sw = __builtin_amdgcn_permlane32_swap(__float_as_uint(s0.x + s0.y), __float_as_uint(q0.x + q0.y), false, false);
    const float t = __uint_as_float(sw[0]) + __uint_as_float(sw[1]);
    __builtin_amdgcn_raw_buffer_store_b32(__float_as_uint(t), rsW, lane4, SB_OFF + l * SB_SZ + (tt2 * 16 + nq * 4 + w) * 256, AUX_SC1);
    SCHED();
  };
  auto publish = [&](GAS unsigned* c, int type) __attribute__((always_inline)) {
    if (tid < 16) __hip_atomic_fetch_add(c + type * 256 * 64, 1u, RLX_AGENT);
  };

  {
    const int l60 = lane < 60 ? lane : 59;
    const unsigned vow = (l60 / 15) * 3840 + ((l60 % 15) / 5) * 1280 + (l60 % 5) * 16;
    const int sow = (40 * mt + 4 * w) * 3840 + hp * 80, w3 = w < 2 ? 8 : 4;
    const int oq = (lane >> 2) & 7, wm = w < 2 ? w : 2;
    const int ku = min((hp >> 2) * 4 + w, 14), uk = ku / 5, tl = ku % 5;
    const unsigned voo = (uk < 2 ? (2 * mt + h) * 20 + 8 * uk + oq : (2 * ((mt & ~1) + (oq >> 2)) + h) * 20 + 16 + (oq & 3)) * 1280 + (lane & 3) * 16;
    const int soo = nq * 320 + (tl == 0 ? 4 : (((mt >> 2) + tl) & 3)) * 64;
    dma1((const unsigned char*)p.wA + sow, vow, ldsw + S_W);
    dma1((const unsigned char*)p.wA + sow + 16 * 3840, vow, ldsw + S_W + 4096);
    dma1((const unsigned char*)p.wA + sow + w3 * 4 * 3840, vow, ldsw + S_W + w3 * 1024);
    dma1((const unsigned char*)p.woA + soo, voo, ldsw + S_W + 12288);
    {
      const unsigned vx = (lane >> 4) * 1280 + (((lane & 15) ^ (4 * w + (lane >> 4))) * 16);
      const unsigned char* xs = (const unsigned char*)p.x0 + (32 * tt2 + 4 * w) * 1280;
#pragma unroll
      for (int i = 0; i < 10; ++i) dma1(xs + (i / 5) * 16 * 1280 + (i % 5) * 256, vx, S_X + w * 5120 + (i / 5) * 20480 + (i % 5) * 1024);
      u64 pc;
      asm volatile("s_getpc_b64 %0" : "=s"(pc));
      const void* gb = w < 2 ? (const void*)((const unsigned char*)p.wphi + hp * 1600) : (w == 2 ? (const void*)(pc & ~127ull) : (const void*)(ws + EP_OFF));
      const unsigned gv = w < 2 ? min(w * 64 + lane, 99) * 16 : (w == 2 ? ((blockIdx.x >> 3) * (CODE_LINES / 32) + (lane < CODE_LINES / 32 ? lane : 0)) * 128 : 0);
      dma1(gb, gv, w < 2 ? S_PH + w * 1024 : (w == 2 ? S_JUNK : S_EP));
    }
    __builtin_amdgcn_raw_buffer_store_b32(0u, rsW, tid < 3 ? tid * 65536 : OOB, CT_OFF + cu * 256, AUX_SC1);
    SCHED();
    dma1((const unsigned char*)p.woB + soo, voo, ldsw + S_W + S_TY + 12288);
    sts128(L_CS + (tid < 136 ? tid : 136) * 16, v4u{0u, 0u, 0u, 0u});
    SCHED();
    const int blk = (lane >> 4) & 1, q = (lane & 15) >> 2, pp = lane & 3;
    int ja0, jb0, jvo;
    const int jvs = w < 3 ? 1024 : 768;
    {
      const int c = 2 * blk + (pp >> 1), hh = pp & 1;
      const int st = w < 3 ? w : (c < 3 ? c : 2), sec = st == 2 ? 0 : st + 1, cc = w < 3 ? c : 4;
      ja0 = S_W + h * 2048 + q * 240 + sec * 80 + hh * 40 + cc * 8;
      jb0 = S_W + 12288 + w * 1024 + (8 * h + q) * 64 + blk * 32 + (pp & 1) * 16 + (pp >> 1) * 8;
      jvo = (w < 3 || r < 24) ? (5 * mt + h) * (jvs >> 1) + r * 16 : 0x70000000;
    }
    const int jso = WT_OFF + hp * WT_IMG + (w == 0 ? L_K : (w == 1 ? L_V : (w == 2 ? L_Q : L_T3)));
    const int jsb = WO_OFF + nq * 204800 + ((mt >> 2) * 50 + tl * 10 + (uk < 2 ? 2 * (mt & 3) + uk : 8 + ((mt & 3) >> 1))) * 1024;
    const int xrow = S_X + (r >> 2) * 5120 + (r & 3) * 256;
    auto xadr = [&](int pc) __attribute__((always_inline)) { return xrow + (pc >> 4) * 1024 + (((pc & 15) ^ (r & 15)) * 16); };
    {
      lane16 = lane * 16; lane8 = lane * 8 + 2048; lane4 = lane * 4;
      at0 = L_T3 + (h * 24 + (r < 24 ? r : 23)) * 16;
      avq = L_V + lane16;
      const int row = h * 8192 + (32 * w + r) * 64, sw = (r >> 1) & 3;
      u0 = row + (0 ^ sw) * 16; u1 = row + (1 ^ sw) * 16; u2 = row + (2 ^ sw) * 16;
      const int un = 2 * blk + (pp >> 1);
      tv0 = (w & 1) * 8192 + (8 * h + q) * 64 + ((un ^ (q >> 1)) * 16) + 8 * (pp & 1);
      tv1 = (w & 1) * 8192 + (8 * h + 4 + q) * 64 + ((un ^ (2 + (q >> 1))) * 16) + 8 * (pp & 1);
      tr0 = tv0 + (w >> 1) * 16384; tr1 = tv1 + (w >> 1) * 16384;
      ksa = r < 24 ? ((w * 2 + h) * 24 + r) * 16 : OOB;
      ksb = lane < 24 ? (w * 24 + r) * 8 : OOB;
      auto rd = [&](int nroles, int u, int& d, int& pv) __attribute__((always_inline)) {
        const int nA = 48 * nroles, c0 = 2 * (u - nA);
        const bool a = u < nA, on = u < 60 * nroles;
        const int role = a ? u / 48 : c0 / 24, e = a ? u % 24 : c0 % 24;
        d = on ? L_KVI + (role & 1) * KVI_HS + e * 96 + (role >> 1) * 40 + (a ? ((u % 48) / 24) * 8 + h * 16 : 32 + h * 96) : L_DUMMY;
        pv = on ? u * 16 + h * 8 * KVP_ONE : OOB;
      };
      rd(2, w * 32 + r, rdA0, pvA); rd(4, w * 64 + r, rdB0, pvB); rd(4, w * 64 + 32 + r, rdB1, rdA1);
      const int c = r >> 3, hi = (r >> 2) & 1, e = r & 3;
      ob0 = hi == h ? L_KVI + h * KVI_HS + (4 * c + e) * 96 : L_ZERO;
      ob1 = hi == h ? L_KVI + h * KVI_HS + (c == 0 ? 16 + e : 20) * 96 : L_ZERO;
      rw0 = L_RED4 + w * 4096 + lane16; rw1 = L_RED + (((w + 1) & 3) * 3 + 2) * 4096 + lane16;
      rw2 = L_RED + (((w + 2) & 3) * 3 + 1) * 4096 + lane16; rw3 = L_RED + (((w + 3) & 3) * 3) * 4096 + lane16;
      rr0 = L_RED + w * 12288 + lane16; rr4 = L_RED4 + (w & 1) * 2048 + lane16;
      csa = L_CS + r * 8;
      x4o = w < 2 ? lane16 : OOB;
      pub_row = (GAS unsigned*)(ws + CT_OFF + (mt * 16 + (tid & 15)) * 256);
      pub_col = (GAS unsigned*)(ws + CT_OFF + ((tid & 15) * 16 + hp) * 256);
      const unsigned one = r < 8 ? ((r & 1) ? 0x3f800000u : 0x00003f80u) : 0u;
      sel = v4u{(r >> 1) == 0 ? one : 0u, (r >> 1) == 1 ? one : 0u, (r >> 1) == 2 ? one : 0u, (r >> 1) == 3 ? one : 0u};
      OPAQUE(lane16); OPAQUE(lane8); OPAQUE(lane4); OPAQUE(at0); OPAQUE(avq); OPAQUE(u0); OPAQUE(u1); OPAQUE(u2); OPAQUE(tr0); OPAQUE(tr1);
      OPAQUE(tv0); OPAQUE(tv1); OPAQUE(ksa); OPAQUE(ksb); OPAQUE(rdA0); OPAQUE(rdA1); OPAQUE(rdB0); OPAQUE(rdB1); OPAQUE(pvA); OPAQUE(pvB);
      OPAQUE(ob0); OPAQUE(ob1); OPAQUE(rw0); OPAQUE(rw1); OPAQUE(rw2); OPAQUE(rw3); OPAQUE(rr0); OPAQUE(rr4); OPAQUE(csa); OPAQUE(x4o);
      OPAQUE(pub_row); OPAQUE(pub_col); OPAQUE(sel);
    }
    VMWAIT(1);
    BAR();
    v4u jf[3], jg;
    auto job_rd = [&](int ty) __attribute__((always_inline)) {
      const int lo = ty * S_TY;
#pragma unroll
      for (int n = 0; n < 3; ++n) jf[n] = trfrag(ja0 + lo + n * 4096, ja0 + lo + n * 4096 + 1024);
      jg = trfrag(jb0 + lo, jb0 + lo + 256);
      SCHED();
    };
    auto job_st = [&](int ty) __attribute__((always_inline)) {
      st128(jf[0], rsW, jvo, jso + ty * 16 * WT_IMG);
      st128(jf[1], rsW, jvo + jvs, jso + ty * 16 * WT_IMG);
      st128(jf[2], rsW, h ? OOB : jvo + 2 * jvs, jso + ty * 16 * WT_IMG);
      st128(jg, rsW, lane16, jsb + ty * 4 * 204800);
    };
    auto colsum = [&]() __attribute__((always_inline)) {
      if (h) jf[2] = v4u{0u, 0u, 0u, 0u};
      f32x2 s0 = {0.f, 0.f}, s1 = {0.f, 0.f};
#pragma unroll
      for (int n = 0; n < 3; ++n) {
        s0 += mk2(bflo(jf[n].x), bfhi(jf[n].x)); s1 += mk2(bflo(jf[n].y), bfhi(jf[n].y));
        s0 += mk2(bflo(jf[n].z), bfhi(jf[n].z)); s1 += mk2(bflo(jf[n].w), bfhi(jf[n].w));
      }
      s0 += s1;
      const unsigned sb = __float_as_uint(s0.x + s0.y);
      const auto sw = __builtin_amdgcn_permlane32_swap(sb, sb, false, false);
      return __uint_as_float(sw[0]) + __uint_as_float(sw[1]);
    };
    {
        const v4u xa = lds128(xadr(nq * 20 + 4 * w + h)), xb = lds128(xadr(nq * 20 + 4 * w + 2 + h)), xc = lds128(xadr(nq * 20 + 16 + 2 * (w & 1) + h));
        {
          const int a0 = xadr(4 * w + 2 * h), a1 = xadr(4 * w + 2 * h + 1);
          v4u t[10];
#pragma unroll
          for (int i = 0; i < 5; ++i) { t[2 * i] = lds128(a0 + i * 1024); t[2 * i + 1] = lds128(a1 + i * 1024); }
          unsigned ones = 0x3f803f80u;
          OPAQUE(ones);
          float s0 = 0.f, s1 = 0.f, q0 = 0.f, q1 = 0.f;
#pragma unroll
          for (int i = 0; i < 10; ++i) {
            s0 = dot2(t[i].x, ones, s0); q0 = dot2(t[i].x, t[i].x, q0); s1 = dot2(t[i].y, ones, s1); q1 = dot2(t[i].y, t[i].y, q1);
            s0 = dot2(t[i].z, ones, s0); q0 = dot2(t[i].z, t[i].z, q0); s1 = dot2(t[i].w, ones, s1); q1 = dot2(t[i].w, t[i].w, q1);
          }
          *LP(f32x2, S_ST + r * 64 + (2 * w + h) * 8) = mk2(s0 + s1, q0 + q1);
        }
        tag = *LP(u64, S_EP) + 1ull;
#pragma unroll
        for (int i = 0; i < 4; ++i) {
          xr[2 * i] = bflo(xa[i]); xr[2 * i + 1] = bfhi(xa[i]);
          xr[8 + 2 * i] = bflo(xb[i]); xr[8 + 2 * i + 1] = bfhi(xb[i]);
          xr4[i] = mk2(bflo(xc[i]), bfhi(xc[i]));
        }
        job_rd(0);
        job_st(0);
        BAR();
        {
          const f32x4 u = (asf4(lds128(S_ST + r * 64)) + asf4(lds128(S_ST + r * 64 + 16))) + (asf4(lds128(S_ST + r * 64 + 32)) + asf4(lds128(S_ST + r * 64 + 48)));
          const float mean = (u[0] + u[2]) * (1.f / 640.f), var = fmaxf(fmaf(u[1] + u[3], 1.f / 640.f, -mean * mean), 0.f);
          const float mb = rbf(mean), rs = __builtin_amdgcn_rcpf(rbf(__builtin_amdgcn_sqrtf(rbf(rbf(var) + 1e-5f))));
          const f32x2 m2 = {mb, mb}, rs2 = {rs, rs};
          auto nrm = [&](f32x2 v) __attribute__((always_inline)) { const unsigned c = pkv(v - m2); return pkv(mk2(bflo(c), bfhi(c)) * rs2); };
          const int vo = XB_OFF + tt2 * 40960 + nq * 10240;
          st128(v4u{nrm(mk2(xr[0], xr[1])), nrm(mk2(xr[2], xr[3])), nrm(mk2(xr[4], xr[5])), nrm(mk2(xr[6], xr[7]))}, rsW, lane16, vo + 2 * w * 1024);
          st128(v4u{nrm(mk2(xr[8], xr[9])), nrm(mk2(xr[10], xr[11])), nrm(mk2(xr[12], xr[13])), nrm(mk2(xr[14], xr[15]))}, rsW, lane16 + 1024, vo + 2 * w * 1024);
          st128(v4u{nrm(xr4[0]), nrm(xr4[1]), nrm(xr4[2]), nrm(xr4[3])}, rsW, x4o, vo + (8 + (w & 1)) * 1024);
          __builtin_amdgcn_raw_buffer_store_b32(h ? __float_as_uint(40.f) : 0u, rsW, lane4, SB_OFF + (tt2 * 16 + nq * 4 + w) * 256, AUX_SC1);
        }
        SCHED();
        dma1((const unsigned char*)p.wB + sow, vow, ldsw + S_W + S_TY);
        dma1((const unsigned char*)p.wB + sow + 16 * 3840, vow, ldsw + S_W + S_TY + 4096);
        dma1((const unsigned char*)p.wB + sow + w3 * 4 * 3840, vow, ldsw + S_W + S_TY + w3 * 1024);
        float csA = colsum();
        OPAQUE(csA);
        VMWAIT(3);
        BAR0();
        if (w == 3) {
          __hip_atomic_store((gu64*)(ws + TG_OFF + (lane * 4 + nq) * 1024 + (cu >> 2) * 8), tag, RLX_AGENT);
          if (lane < 16) __hip_atomic_store((gu64*)(ws + TG_OFF + (mt * 16 + lane) * 1024 + (64 + hp) * 8), tag, RLX_AGENT);
        }
        {
          const int c = r >> 3, hi = (r >> 2) & 1, e = r & 3;
#pragma unroll
          for (int tile = 0; tile < 2; ++tile) {
            const int f = tile == 0 ? 4 * c + e : 16 + e;
            const unsigned m0 = ((hi == h) && (tile == 0 || c == 0)) ? ~0u : 0u, m1 = wm < 2 ? m0 : 0u;
            unsigned v[8];
#pragma unroll
            for (int j = 0; j < 8; ++j) v[j] = *LP(u16, S_PH + h * 800 + wm * 320 + f * 2 + j * 40);
            sts128(L_WPH + (tile * 3 + wm) * 1024 + lane16,
                   v4u{(v[0] | (v[1] << 16)) & m0, (v[2] | (v[3] << 16)) & m0, (v[4] | (v[5] << 16)) & m1, (v[6] | (v[7] << 16)) & m1});
          }
        }
        DRAIN();
        BAR();
        {
          const int k = min(32 * (w >> 1) + r, 39);
          const int fa = S_W + S_TY + (k >> 2) * 1024 + (k & 3) * 240 + (w & 1) * 80 + h * 40;
          v2u t[5];
          v4u wf[6];
#pragma unroll
          for (int i = 0; i < 5; ++i) t[i] = *LP(v2u, fa + i * 8);
#pragma unroll
          for (int i = 0; i < 6; ++i) wf[i] = lds128(L_WPH + i * 1024 + lane16);
          v4u b0 = {t[0].x, t[0].y, t[1].x, t[1].y}, b1 = {t[2].x, t[2].y, t[3].x, t[3].y}, b2 = {t[4].x, t[4].y, 0u, 0u};
          OPAQUE(b0); OPAQUE(b1); OPAQUE(b2);
          f32x16 d0, d1;
          MFMA0N(d0, wf[0], b0); MFMA0(d1, wf[3], b0);
          MFMA(d0, wf[1], b1);   MFMA(d1, wf[4], b1);
          MFMA(d0, wf[2], b2);   MFMA(d1, wf[5], b2);
          MFENCE2(d0, d1);
#pragma unroll
          for (int i = 0; i < 4; ++i) *LP(v2u, fa + i * 8) = v2u{pk2(d0[4 * i], d0[4 * i + 1]), pk2(d0[4 * i + 2], d0[4 * i + 3])};
          *LP(v2u, fa + 32) = v2u{pk2(d1[0], d1[1]), pk2(d1[2], d1[3])};
          BAR();
        }
        job_rd(1);
        if (w == 3) wait_tags(ws, TG_OFF + cu * 1024 + lane * 8, TG_OFF + cu * 1024 + (64 + (lane & 15)) * 8, tag);
        BAR();
#pragma nounroll
        for (int k = 0; k < 21; ++k) DMAKS(rsW, lane16, WT_OFF + hp * WT_IMG + ldsw, ldsw, k * 4096);
        job_st(1);
        const float csB = colsum();
        __builtin_amdgcn_raw_buffer_store_b32(__float_as_uint(h ? csB : csA), rsW, h * 16 * 8192 + r * 4, CS_OFF + (hp * 16 + mt) * 512 + w * 128, AUX_SC1);
        SCHED();
    }
#pragma unroll
    for (int k = 0; k < 7; ++k) DMAK(rsW, lane16, WT_OFF + hp * WT_IMG + ldsw, ldsw, (82 + 4 * k) * 1024);
    VMWAIT(12);
    BAR();
#pragma unroll
    for (int k = 0; k < QX; ++k) DMAK(rsW, lane16, WT_OFF + hp * WT_IMG + ldsw, ldsw, L_Q + k * 4096);
  }

  auto layer = [&](auto TB, const int l) __attribute__((always_inline)) {
    constexpr bool isB = decltype(TB)::value != 0;
    constexpr int nroles = isB ? 4 : 2;
    const int xso = XB_OFF + l * XB_SZ + tt * 40960;
    const int wcur = WT_OFF + ((isB ? 16 : 0) + hp) * WT_IMG + w * 1024, wnext = WT_OFF + ((isB ? 0 : 16) + hp) * WT_IMG + w * 1024;
    const int sso = SB_OFF + l * SB_SZ + tt * 4096;
    const int wov = WO_OFF + ((isB ? 4 : 0) + nq) * 204800 + w * 51200;
    const int kvp = KVP_OFF + l * KVP_SZ + hp * 16 * KVP_ONE;
    const unsigned want = 16u * (l + 1);
    v4u xf[40], wo[50];
#define LDX(ks) ld128(rsW, lane16 + ((ks) & 3) * 1024, xso + ((ks) >> 2) * 4096)
#define LDWO(i) wo[i] = ld128(rsW, lane16 + ((i) & 3) * 1024, wov + ((i) >> 2) * 4096)
#pragma unroll
    for (int ks = 0; ks < XW0; ++ks) { xf[ks] = LDX(ks); SCHED(); }
    f32x16 aK, aV, aQ, aT;
    float rs;
    v4u mb, csq, csv;
    constexpr int VD = 4;
    v4u fv[VD + 1];
    auto vstep = LAMBDA(I) {
      constexpr int i = decltype(I)::value;
      if constexpr (i == 0) MFMA0B(aV, fv[0], xf[0]);
      else if constexpr (i < 40) MFMAB(aV, fv[i % (VD + 1)], xf[i]);
      else if constexpr (i == 40) MFMA(aV, csv, mb);
      if constexpr (i + VD < 40) fv[(i + VD) % (VD + 1)] = lds128(avq + (i + VD) * 1024);
    };
    {
      v4u fk[4], ft[4];
      v2u cs[4];
      float sp[16];
#pragma unroll
      for (int ks = 0; ks < 3; ++ks) { fk[ks] = lds128(lane16 + ks * 1024); ft[ks] = lds128(at0 + ks * 768); }
#pragma unroll
      for (int ks = 0; ks < VD; ++ks) fv[ks] = lds128(avq + ks * 1024);
      SCHED();
      float mean = 0.f, var = 0.f;
      sfor<0, 40>(LAMBDA(KS) {
        constexpr int ks = decltype(KS)::value;
        constexpr int c = ks % 4, n = (ks + 3) % 4;
        if constexpr (ks == 0) MFMA0B(aK, fk[c], xf[ks]); else MFMAB(aK, fk[c], xf[ks]);
        if constexpr (ks + 3 < 40) fk[n] = lds128(lane16 + (ks + 3) * 1024);
        if constexpr (XW0 + ks < 40) xf[XW0 + ks] = LDX(XW0 + ks);
        if constexpr (ks == 32) { sp[0] += sp[1]; sp[2] += sp[3]; sp[4] += sp[5]; sp[6] += sp[7]; }
        if constexpr (ks == 33) { sp[0] += sp[2]; sp[4] += sp[6]; sp[8] += sp[10]; sp[12] += sp[14]; }
        if constexpr (ks == 34) {
          const auto sw = __builtin_amdgcn_permlane32_swap(__float_as_uint(sp[0]), __float_as_uint(sp[0]), false, false);
          mean = __uint_as_float(sw[0]) * (1.f / 640.f); var = __uint_as_float(sw[1]);
        }
        if constexpr (ks == 35) { const unsigned nm = h ? 0u : pk2(-mean, -mean); mb = v4u{nm, nm, 0u, 0u}; }
        if constexpr (ks == 36) { var = __builtin_amdgcn_sqrtf(var); var = rbf(var); }
        SCHED();
        if constexpr (ks == 0) MFMA0B(aT, ft[c], xf[ks]); else MFMAB(aT, ft[c], xf[ks]);
        if constexpr (ks + 3 < 40) ft[n] = lds128(at0 + (ks + 3) * 768);
        if constexpr (ks >= SP0 && ks < SP0 + 16) sp[ks - SP0] = __uint_as_float(__builtin_amdgcn_raw_buffer_load_b32(rsW, lane4 + (ks - SP0) * 256, sso, AUX_SC1));
        if constexpr (ks == 6) BAR0();
        if constexpr (ks >= S0 && ((ks - S0) * N0) / (40 - S0) != ((ks + 1 - S0) * N0) / (40 - S0)) LDWO(((ks - S0) * N0) / (40 - S0));
        if constexpr (ks == 30) {
#pragma unroll
          for (int i = 0; i < 4; ++i) cs[i] = *LP(v2u, csa + (isB ? 1024 : 0) + i * 256);
        }
        if constexpr (ks == 32) { sp[8] += sp[9]; sp[10] += sp[11]; sp[12] += sp[13]; sp[14] += sp[15]; }
        if constexpr (ks == 33) { sp[0] += sp[4]; sp[8] += sp[12]; sp[0] += sp[8]; }
        if constexpr (ks == 34) { var = fmaxf(fmaf(var, 1.f / 640.f, -mean * mean), 0.f); }
        if constexpr (ks == 35) { var = rbf(var) + 1e-5f; var = rbf(var); }
        if constexpr (ks == 36) { rs = __builtin_amdgcn_rcpf(var); }
        SCHED();
        if constexpr (ks < NV0) { vstep(IC<ks>{}); SCHED(); }
        if constexpr (ks == 37) {
          const v4u c0 = {cs[0].x, cs[0].y, 0u, 0u}, c3 = {cs[3].x, cs[3].y, 0u, 0u};
          csv = v4u{cs[1].x, cs[1].y, 0u, 0u}; csq = v4u{cs[2].x, cs[2].y, 0u, 0u};
          OPAQUE(csv); OPAQUE(csq);
          MFMAN(aK, c0, mb); MFMAN(aT, c3, mb);
          SCHED();
        }
      });
    }
    {
      BAR0();
      auto tick = LAMBDA(N) {
        constexpr int n = decltype(N)::value;
        vstep(IC<NV0 + n>{});
        if constexpr (n < QT && (n * (10 - QX)) / QT != ((n + 1) * (10 - QX)) / QT) DMAK(rsW, lane16, wcur, ldsw, L_Q + (QX + (n * (10 - QX)) / QT) * 4096);
        SCHED();
      };
      tick(IC<0>{});
      tick(IC<1>{});
      tick(IC<2>{});
      OPAQUE(aK); OPAQUE(aT);
#define KVAL(i) ((i) < 16 ? aK[(i) & 15] : aT[(i) & 3])
#define IMGST(off, v) do { \
        sts128(u0 + (off), v4u{v[0], v[1], v[2], v[3]}); sts128(u1 + (off), v4u{v[4], v[5], v[6], v[7]}); sts128(u2 + (off), v4u{v[8], v[9], 0u, 0u}); } while (0)
      if constexpr (!isB) {
        const f32x2 rs2 = {rs, rs}, rl2 = {rs * LOG2E, rs * LOG2E}, eps = {1e-6f, 1e-6f};
        unsigned pos[10];
        f32x2 e[10], t[10];
#define KV2(i) mk2(KVAL(2 * (i)), KVAL(2 * (i) + 1))
        sfor<0, 15>(LAMBDA(I) {
          constexpr int i = decltype(I)::value;
          if constexpr (i < 10) e[i] = KV2(i) * rl2;
          if constexpr (i >= 1 && i < 11) { e[i - 1].x = exp2c(e[i - 1].x); e[i - 1].y = exp2c(e[i - 1].y); }
          if constexpr (i >= 2 && i < 12) e[i - 2] += eps;
          if constexpr (i >= 3 && i < 13) t[i - 3] = __builtin_elementwise_fma(KV2(i - 3), rs2, e[i - 3]);
          if constexpr (i >= 4 && i < 14) { t[i - 4].x = fmaxf(t[i - 4].x, e[i - 4].x); t[i - 4].y = fmaxf(t[i - 4].y, e[i - 4].y); }
          if constexpr (i >= 5) pos[i - 5] = pkv(t[i - 5]);
          tick(IC<3 + i>{});
        });
        IMGST(L_IPK, pos);
        sfor<18, 41 - NV0>(LAMBDA(I) { tick(I); });
      } else {
        unsigned pos[10], neg[10];
#define PVAL(i) KVAL(i)
        float mx0, mn0, mx1, mn1;
        asm("v_max3_f32 %0, %4, %5, %6\n\tv_min3_f32 %1, %4, %5, %6\n\tv_max3_f32 %2, %7, %8, %9\n\tv_min3_f32 %3, %7, %8, %9"
            : "=&v"(mx0), "=&v"(mn0), "=&v"(mx1), "=&v"(mn1) : "v"(PVAL(0)), "v"(PVAL(1)), "v"(PVAL(2)), "v"(PVAL(3)), "v"(PVAL(4)), "v"(PVAL(5)));
        tick(IC<3>{});
        sfor<0, 3>(LAMBDA(I) {
          constexpr int i = decltype(I)::value;
          asm("v_max3_f32 %0, %0, %4, %5\n\tv_min3_f32 %1, %1, %4, %5\n\tv_max3_f32 %2, %2, %6, %7\n\tv_min3_f32 %3, %3, %6, %7"
              : "+v"(mx0), "+v"(mn0), "+v"(mx1), "+v"(mn1) : "v"(PVAL(6 + 4 * i)), "v"(PVAL(7 + 4 * i)), "v"(PVAL(8 + 4 * i)), "v"(PVAL(9 + 4 * i)));
          tick(IC<4 + i>{});
        });
        asm("v_max3_f32 %0, %0, %4, %5\n\tv_min3_f32 %1, %1, %4, %5\n\tv_max_f32 %0, %0, %2\n\tv_min_f32 %1, %1, %3"
            : "+v"(mx0), "+v"(mn0) : "v"(mx1), "v"(mn1), "v"(PVAL(18)), "v"(PVAL(19)));
        const float cl = rs * LOG2E, mxl = -mx0 * cl, mnl = mn0 * cl;
        tick(IC<7>{});
        const f32x2 l2 = {cl, cl}, nl2 = {-cl, -cl}, mx2 = {mxl, mxl}, mn2 = {mnl, mnl};
        f32x2 ea[10], eb[10], sa, sb;
        sfor<0, 12>(LAMBDA(I) {
          constexpr int i = decltype(I)::value;
          if constexpr (i < 10) { const f32x2 v = mk2(PVAL(2 * i), PVAL(2 * i + 1)); ea[i] = __builtin_elementwise_fma(v, l2, mx2); eb[i] = __builtin_elementwise_fma(v, nl2, mn2); }
          if constexpr (i >= 1 && i < 11) {
            ea[i - 1].x = __builtin_amdgcn_exp2f(ea[i - 1].x); ea[i - 1].y = __builtin_amdgcn_exp2f(ea[i - 1].y);
            eb[i - 1].x = __builtin_amdgcn_exp2f(eb[i - 1].x); eb[i - 1].y = __builtin_amdgcn_exp2f(eb[i - 1].y);
          }
          if constexpr (i == 3) { sa = ea[0] + ea[1]; sb = eb[0] + eb[1]; }
          if constexpr (i > 3) { sa += ea[i - 2]; sb += eb[i - 2]; }
          tick(IC<8 + i>{});
        });
        const float ra = __builtin_amdgcn_rcpf(sa.x + sa.y), rb = __builtin_amdgcn_rcpf(sb.x + sb.y);
        const f32x2 ra2 = {ra, ra}, rb2 = {rb, rb};
        tick(IC<20>{});
#pragma unroll
        for (int i = 0; i < 10; ++i) { ea[i] *= ra2; eb[i] *= rb2; }
        tick(IC<21>{});
#pragma unroll
        for (int i = 0; i < 10; ++i) { pos[i] = pkv(ea[i]); neg[i] = pkv(eb[i]); }
        tick(IC<22>{});
        IMGST(L_IPK, pos);
        IMGST(L_IPK + 16384, neg);
      }
      SCHED();
      const f32x2 rs2 = {rs, rs};
      const unsigned v8 = pkv(mk2(aT[4], aT[5]) * rs2), v9 = pkv(mk2(aT[6], aT[7]) * rs2);
      sts128(u2 + L_IV, v4u{v8, v9, 0x00003f80u, 0u});
      SCHED();
      asm volatile("s_nop 6" : "+v"(aV));
      f32x2 t[8];
#pragma unroll
      for (int i = 0; i < 8; ++i) t[i] = mk2(aV[2 * i], aV[2 * i + 1]) * rs2;
      SCHED();
      sts128(u0 + L_IV, v4u{pkv(t[0]), pkv(t[1]), pkv(t[2]), pkv(t[3])});
      sts128(u1 + L_IV, v4u{pkv(t[4]), pkv(t[5]), pkv(t[6]), pkv(t[7])});
      DRAIN();
      BAR();
    }
    constexpr int QD = 4, Q1 = 16;
    v4u fq[QD + 1];
    unsigned seen;
    if (w < nroles) {
      v4u fa[8], fb[8];
      f32x16 d;
      sfor<0, 10>(LAMBDA(S) {
        constexpr int s = decltype(S)::value;
        if constexpr (s < 8) { fa[s] = trfrag(tr0 + s * 1024, tr1 + s * 1024); fb[s] = trfrag(tv0 + L_IV + s * 1024, tv1 + L_IV + s * 1024); }
        if constexpr (s == 2) MFMA0(d, fa[0], fb[0]);
        if constexpr (s > 2) MFMA(d, fa[s - 2], fb[s - 2]);
        SCHED();
      });
      MFENCE1(d);
      st128(v4u{pk2(d[0], d[1]), pk2(d[2], d[3]), pk2(d[4], d[5]), pk2(d[6], d[7])}, rsW, ksa, kvp + mt * KVP_ONE);
      __builtin_amdgcn_raw_buffer_store_b64(v2u{pk2(d[8], d[9]), pk2(d[10], d[11])}, rsW, ksb, kvp + mt * KVP_ONE + nroles * 768, AUX_SC1);
      SCHED();
    }
    {
#pragma unroll
      for (int ks = 0; ks < QD; ++ks) fq[ks] = lds128(avq + (L_Q - L_V) + ks * 1024);
      SCHED();
      MFMA0(aQ, csq, mb);
      int aq2 = avq + (L_Q - L_V) + 20480;
      OPAQUE(aq2);
      sfor<0, 40>(LAMBDA(KS) {
        constexpr int ks = decltype(KS)::value;
        MFMAB(aQ, fq[ks % (QD + 1)], xf[ks]);
        if constexpr (ks + QD < 20) fq[(ks + QD) % (QD + 1)] = lds128(avq + (L_Q - L_V) + (ks + QD) * 1024);
        else if constexpr (ks + QD < 40) fq[(ks + QD) % (QD + 1)] = lds128(aq2 + (ks + QD - 20) * 1024);
        if constexpr (ks < 16 && (ks * N2A) / 16 != ((ks + 1) * N2A) / 16) LDWO(W1 + (ks * N2A) / 16);
        if constexpr (ks >= 16 && (ks & 1) == 0 && (ks - 16) / 2 < N2B) LDWO(W2 + (ks - 16) / 2);
        SCHED();
        if constexpr (ks == Q1 - 1) {
          VMWAITN(N2A);
          BAR0();
          publish(pub_col, 1);
          asm volatile("s_nop 3");
          SCHED();
        }
      });
    }
    v4u p0, p1, p2, p3, p4;
    {
      constexpr int NP = isB ? 16 : 8;
      v4u pp[NP];
      f32x16 d, dd;
#define LDP(m) pp[m] = ld128(rsW, isB ? (((m) & 1) ? rdA1 : pvB) : pvA, kvp + (isB ? (m) >> 1 : (m)) * KVP_ONE)
#define RED(m) do { \
        if constexpr (isB && ((m) & 1)) { if constexpr ((m) == 1) MFMA0(dd, sel, pp[m]); else MFMA(dd, sel, pp[m]); } \
        else { if constexpr ((m) == 0) MFMA0(d, sel, pp[0]); else MFMA(d, sel, pp[m]); } \
        if constexpr (((m) * N3B) / NP != (((m) + 1) * N3B) / NP) LDWO(W3B + ((m) * N3B) / NP); SCHED(); } while (0)
      f32x2 a[10];
      const f32x2 rs2 = {rs, rs};
      if constexpr (!isB) {
        MFENCE1(aQ);
#pragma unroll
        for (int i = 0; i < 8; ++i) a[i] = mk2(aQ[2 * i], aQ[2 * i + 1]);
        a[8] = mk2(aT[8], aT[9]); a[9] = mk2(aT[10], aT[11]);
        const f32x2 rl2 = {rs * LOG2E, rs * LOG2E}, eps = {1e-6f, 1e-6f};
        f32x2 e[10], t[10];
#pragma unroll
        for (int i = 0; i < 10; ++i) e[i] = a[i] * rl2;
        SCHED();
#pragma unroll
        for (int i = 0; i < 10; ++i) { e[i].x = exp2c(e[i].x); e[i].y = exp2c(e[i].y); }
        seen = poll_ctr(ws, 1, cu);
        SCHED();
#pragma unroll
        for (int i = 0; i < 10; ++i) e[i] = e[i] + eps;
        SCHED();
#pragma unroll
        for (int i = 0; i < 10; ++i) t[i] = __builtin_elementwise_fma(a[i], rs2, e[i]);
        SCHED();
#pragma unroll
        for (int i = 0; i < 10; ++i) { t[i].x = fmaxf(t[i].x, e[i].x); t[i].y = fmaxf(t[i].y, e[i].y); }
        SCHED();
        p0 = v4u{pkv(t[0]), pkv(t[1]), pkv(t[2]), pkv(t[3])}; p1 = v4u{pkv(t[4]), pkv(t[5]), pkv(t[6]), pkv(t[7])}; p2 = v4u{pkv(t[8]), pkv(t[9]), 0u, 0u};
        OPAQUE(p0); OPAQUE(p1); OPAQUE(p2);
        SCHED();
        wait_ctr(ws, 1, cu, want, seen, budget);
#pragma unroll
        for (int m = 0; m < 8; ++m) { LDP(m); SCHED(); }
#pragma unroll
        for (int i = W3; i < W3B; ++i) { LDWO(i); SCHED(); }
        sfor<0, 8>(LAMBDA(M) { RED(decltype(M)::value); });
        MFENCE1(d);
        *LP(v2u, rdA0) = v2u{pk2(d[0], d[1]), pk2(d[2], d[3])};
      } else {
        MFENCE1(aQ);
#pragma unroll
        for (int i = 0; i < 8; ++i) a[i] = mk2(aQ[2 * i], aQ[2 * i + 1]);
        a[8] = mk2(aT[8], aT[9]); a[9] = mk2(aT[10], aT[11]);
        float mx0, mn0, mx1, mn1;
        asm("v_max3_f32 %0, %4, %5, %6\n\tv_min3_f32 %1, %4, %5, %6\n\tv_max3_f32 %2, %7, %8, %9\n\tv_min3_f32 %3, %7, %8, %9\n\t"
            "v_max3_f32 %0, %0, %10, %11\n\tv_min3_f32 %1, %1, %10, %11\n\tv_max3_f32 %2, %2, %12, %13\n\tv_min3_f32 %3, %3, %12, %13\n\t"
            "v_max3_f32 %0, %0, %14, %15\n\tv_min3_f32 %1, %1, %14, %15\n\tv_max3_f32 %2, %2, %16, %17\n\tv_min3_f32 %3, %3, %16, %17\n\t"
            "v_max3_f32 %0, %0, %18, %19\n\tv_min3_f32 %1, %1, %18, %19\n\tv_max3_f32 %2, %2, %20, %21\n\tv_min3_f32 %3, %3, %20, %21\n\t"
            "v_max3_f32 %0, %0, %22, %23\n\tv_min3_f32 %1, %1, %22, %23\n\tv_max_f32 %0, %0, %2\n\tv_min_f32 %1, %1, %3"
            : "=&v"(mx0), "=&v"(mn0), "=&v"(mx1), "=&v"(mn1)
            : "v"(a[0].x), "v"(a[0].y), "v"(a[1].x), "v"(a[1].y), "v"(a[2].x), "v"(a[2].y), "v"(a[3].x), "v"(a[3].y), "v"(a[4].x), "v"(a[4].y),
              "v"(a[5].x), "v"(a[5].y), "v"(a[6].x), "v"(a[6].y), "v"(a[7].x), "v"(a[7].y), "v"(a[8].x), "v"(a[8].y), "v"(a[9].x), "v"(a[9].y));
        const float cl = rs * LOG2E, mxl = -mx0 * cl, mnl = mn0 * cl;
        const f32x2 l2 = {cl, cl}, nl2 = {-cl, -cl}, mx2 = {mxl, mxl}, mn2 = {mnl, mnl};
        f32x2 ea[10], eb[10];
        SCHED();
#pragma unroll
        for (int i = 0; i < 10; ++i) { ea[i] = __builtin_elementwise_fma(a[i], l2, mx2); eb[i] = __builtin_elementwise_fma(a[i], nl2, mn2); }
        seen = poll_ctr(ws, 1, cu);
        SCHED();
#pragma unroll
        for (int i = 0; i < 10; ++i) {
          ea[i].x = __builtin_amdgcn_exp2f(ea[i].x); ea[i].y = __builtin_amdgcn_exp2f(ea[i].y);
          eb[i].x = __builtin_amdgcn_exp2f(eb[i].x); eb[i].y = __builtin_amdgcn_exp2f(eb[i].y);
        }
#pragma unroll
        for (int i = 0; i < 10; ++i) { OPAQUE(ea[i]); OPAQUE(eb[i]); }
        SCHED();
        wait_ctr(ws, 1, cu, want, seen, budget);
        f32x2 sa, sb, ra2, rb2;
        unsigned pa[10], pb[10];
        sfor<0, 16>(LAMBDA(M) {
          constexpr int m = decltype(M)::value;
          LDP(m);
          if constexpr (m == 2) { sa = ea[0] + ea[1]; sb = eb[0] + eb[1]; }
          if constexpr (m > 2 && m <= 10) { sa += ea[m - 1]; sb += eb[m - 1]; }
          if constexpr (m == 10) {
            const float ra = __builtin_amdgcn_rcpf(sa.x + sa.y), rb = __builtin_amdgcn_rcpf(sb.x + sb.y);
            ra2 = mk2(ra, ra); rb2 = mk2(rb, rb);
          }
          if constexpr (m > 10) {
            constexpr int i = 2 * (m - 11);
            ea[i] *= ra2; eb[i] *= rb2; ea[i + 1] *= ra2; eb[i + 1] *= rb2;
            pa[i] = pkv(ea[i]); pb[i] = pkv(eb[i]); pa[i + 1] = pkv(ea[i + 1]); pb[i + 1] = pkv(eb[i + 1]);
          }
          SCHED();
          if constexpr (m >= RLAG) RED(m - RLAG);
        });
        p0 = v4u{pa[0], pa[1], pa[2], pa[3]}; p1 = v4u{pa[4], pa[5], pa[6], pa[7]}; p2 = v4u{pa[8], pa[9], pb[0], pb[1]};
        p3 = v4u{pb[2], pb[3], pb[4], pb[5]}; p4 = v4u{pb[6], pb[7], pb[8], pb[9]};
        OPAQUE(p0); OPAQUE(p1); OPAQUE(p2); OPAQUE(p3); OPAQUE(p4);
#pragma unroll
        for (int i = W3; i < W3B; ++i) { LDWO(i); SCHED(); }
        sfor<16 - RLAG, 16>(LAMBDA(M) { RED(decltype(M)::value); });
        MFENCE2(d, dd);
        *LP(v2u, rdB0) = v2u{pk2(d[0], d[1]), pk2(d[2], d[3])};
        *LP(v2u, rdB1) = v2u{pk2(dd[0], dd[1]), pk2(dd[2], dd[3])};
      }
      BAR();
    }
    if (!isB && l == 0) {
      DMAK(rsW, lane16, CS_OFF + hp * 8192 + ldsw - L_V, ldsw, L_V);
      DMAK(rsW, lane16, CS_OFF + hp * 8192 + ldsw - L_V, ldsw, L_V + 4096);
      DMAK(rsW, lane16, CS_OFF + (15 + hp) * 8192 + ldsw - L_V, ldsw, L_V + 8192);
      DMAK(rsW, lane16, CS_OFF + (15 + hp) * 8192 + ldsw - L_V, ldsw, L_V + 12288);
    }
    {
      constexpr int NS = isB ? 5 : 3;
      v4u fa[5], fb[5];
#pragma unroll
      for (int s = 0; s < NS; ++s) {
        if (!isB && s == 2) { const v2u t = *LP(v2u, ob1 + 32); fb[s] = v4u{t.x, t.y, 0u, 0u}; } else fb[s] = lds128(ob1 + 16 * s);
      }
#pragma unroll
      for (int s = 0; s < NS; ++s) {
        if (!isB && s == 2) { const v2u t = *LP(v2u, ob0 + 32); fa[s] = v4u{t.x, t.y, 0u, 0u}; } else fa[s] = lds128(ob0 + 16 * s);
      }
      SCHED();
      f32x16 n0, n1;
      MFMA0(n1, fb[0], p0); MFMA(n1, fb[1], p1); MFMA(n1, fb[2], p2);
      if constexpr (isB) { MFMA(n1, fb[3], p3); MFMA(n1, fb[4], p4); }
      MFMA0(n0, fa[0], p0); MFMA(n0, fa[1], p1); MFMA(n0, fa[2], p2);
      if constexpr (isB) { MFMA(n0, fa[3], p3); MFMA(n0, fa[4], p4); }
      OPAQUE(n1);
      const float inv = __builtin_amdgcn_rcpf(n1[4] + 1e-6f);
      const f32x2 iv = {inv, inv};
      const int ob = OB_OFF + l * XB_SZ + (tt * 16 + hp) * 2560;
      __builtin_amdgcn_raw_buffer_store_b64(v2u{pkv(mk2(n1[0], n1[1]) * iv), pkv(mk2(n1[2], n1[3]) * iv)}, rsW, lane8, ob, AUX_SC1);
      SCHED();
      asm volatile("s_nop 3" : "+v"(n0));
      f32x2 t[8];
#pragma unroll
      for (int i = 0; i < 8; ++i) t[i] = mk2(n0[2 * i], n0[2 * i + 1]) * iv;
      SCHED();
      st128(v4u{pkv(t[0]), pkv(t[1]), pkv(t[2]), pkv(t[3])}, rsW, lane16, ob);
      st128(v4u{pkv(t[4]), pkv(t[5]), pkv(t[6]), pkv(t[7])}, rsW, lane16 + 1024, ob);
      SCHED();
    }
#pragma unroll
    for (int i = W4; i < W5; ++i) { LDWO(i); SCHED(); }
    VMWAITN(N4);
    BAR0();
    publish(pub_row, 2);
#pragma unroll
    for (int i = W5; i < W6; ++i) { LDWO(i); SCHED(); }
    if (!isB && l == 0 && w >= 2) cs_finish(w - 2, lane, L_V + (w - 2) * 8192);
    const bool more = !isB || l < 3;
    const rsrc_t rsD = __builtin_amdgcn_make_buffer_rsrc((void*)ws, 0, more ? WS_BYTES : 0, 0x00020000);
    wait_ctr(ws, 2, cu, want, poll_ctr(ws, 2, cu), budget);
    f32x2 s4, q4;
    {
      v4u of[10];
      const int ob = OB_OFF + l * XB_SZ + (tt2 * 16 + 4 * w) * 2560;
      v2u oh[4];
#pragma unroll
      for (int a = 0; a < 4; ++a) {
        of[2 * a] = ld128(rsW, lane16, ob + a * 2560); SCHED();
        of[2 * a + 1] = ld128(rsW, lane16 + 1024, ob + a * 2560); SCHED();
      }
#pragma unroll
      for (int a = 0; a < 4; ++a) oh[a] = __builtin_amdgcn_raw_buffer_load_b64(rsW, lane8, ob + a * 2560, AUX_SC1);
      SCHED();
      of[8] = v4u{oh[0].x, oh[0].y, oh[1].x, oh[1].y};
      of[9] = v4u{oh[2].x, oh[2].y, oh[3].x, oh[3].y};
      f32x16 acc[4];
      v4u rd[12], r4[8];
      f32x4 sm[4];
      sfor<0, 10>(LAMBDA(U) {
        constexpr int u = decltype(U)::value;
        if constexpr (u == 0) { MFMA0A(acc[0], wo[0], of[0]); MFMA0A(acc[1], wo[10], of[0]); }
        else {
          if constexpr (u == 8) asm volatile("s_nop 1" : "+v"(of[8]), "+v"(of[9]));
          MFMAA(acc[0], wo[u], of[u]);
          if constexpr (u == 10 - D0) { OPAQUE(of[8]); OPAQUE(of[9]); }
          if constexpr (u >= 10 - D0) DMAK(rsD, lane16, wnext, ldsw, (u - (10 - D0)) * 4096);
          MFMAA(acc[1], wo[10 + u], of[u]);
        }
        SCHED();
      });
      sfor<20, 50>(LAMBDA(I) {
        constexpr int i = decltype(I)::value, tl = i / 10, u = i % 10;
        if constexpr (tl < 4) { if constexpr (u == 0) MFMA0A(acc[tl], wo[i], of[0]); else MFMAA(acc[tl], wo[i], of[u]); }
        else MFMA(xr, wo[i], of[u]);
        constexpr int st = tl == 2 ? (u < 6 ? 0 : 1) : tl - 1;
        constexpr int q0 = tl == 2 ? (u < 6 ? u - 2 : u - 6) : (tl == 3 ? u - 2 : 2 * (u - 2));
        sfor<0, (tl == 4 ? 2 : 1)>(LAMBDA(J) {
          constexpr int qd = q0 + decltype(J)::value;
          if constexpr (qd >= 0 && qd < 4) {
            if constexpr (qd == 0) OPAQUE(acc[st]);
            sts128((st == 0 ? rw0 : (st == 1 ? rw1 : (st == 2 ? rw2 : rw3))) + qd * 1024,
                   __builtin_bit_cast(v4u, f32x4{acc[st][4 * qd], acc[st][4 * qd + 1], acc[st][4 * qd + 2], acc[st][4 * qd + 3]}));
          }
        });
        if constexpr (i >= 21 && i < DE && ((i - 21) * D1) / (DE - 21) != ((i - 20) * D1) / (DE - 21)) DMAK(rsD, lane16, wnext, ldsw, (D0 + ((i - 21) * D1) / (DE - 21)) * 4096);
        if constexpr (i == 37) BAR();
        if constexpr (i == 37 || i == 38) {
#pragma unroll
          for (int k = 0; k < 4; ++k) { constexpr int b = (i - 37) * 4; r4[b + k] = lds128(rr4 + ((b + k) >> 1) * 4096 + ((b + k) & 1) * 1024); }
        }
        if constexpr (i == 39 || i == 40) {
#pragma unroll
          for (int k = 0; k < 4; ++k) rd[(i - 38) * 4 + k] = lds128(rr0 + ((i - 38) * 4 + k) * 1024);
        }
        if constexpr (i == 44) {
          const f32x4 t0 = (asf4(r4[0]) + asf4(r4[2])) + (asf4(r4[4]) + asf4(r4[6]));
          xr4[0] += mk2(t0[0], t0[1]); xr4[1] += mk2(t0[2], t0[3]);
        }
        if constexpr (i == 45) {
          BAR();
#pragma unroll
          for (int k = 0; k < 4; ++k) rd[k] = lds128(rr0 + k * 1024);
        }
        if constexpr (i == 46) {
          const f32x4 t1 = (asf4(r4[1]) + asf4(r4[3])) + (asf4(r4[5]) + asf4(r4[7]));
          xr4[2] += mk2(t1[0], t1[1]); xr4[3] += mk2(t1[2], t1[3]);
        }
        if constexpr (i == 47) {
#pragma unroll
          for (int k = 0; k < 4; ++k) sm[k] = asf4(rd[4 + k]) + asf4(rd[8 + k]);
        }
        if constexpr (i == 48) {
#pragma unroll
          for (int k = 0; k < 4; ++k) sm[k] += asf4(rd[k]);
        }
        SCHED();
      });
      store_x4(rsD, l + 1, s4, q4);
      OPAQUE(s4); OPAQUE(q4);
      SCHED();
      asm volatile("s_nop 3");
      OPAQUE(xr);
#pragma unroll
      for (int i = 0; i < 16; ++i) xr[i] += sm[i >> 2][i & 3];
    }
    if (more) {
      store_x(l + 1, s4, q4);
      constexpr int NE = 21 - D0 - D1, NX = NE + 7 + QX;
      static_assert(D2 >= NE && D2 <= NX, "");
      auto xdma = LAMBDA(K) {
        constexpr int k = decltype(K)::value;
        DMAK(rsW, lane16, wnext, ldsw, k < NE ? (D0 + D1 + k) * 4096 : (k < NE + 7 ? (82 + 4 * (k - NE)) * 1024 : L_Q + (k - NE - 7) * 4096));
      };
      sfor<0, NE>(xdma);
      if constexpr (D2 > NE) BAR0();
      sfor<NE, D2>(xdma);
      VMWAITN(D2);
      BAR0();
      publish(pub_row, 0);
      sfor<D2, NX>(xdma);
      if (w == 0) wait_ctr(ws, 0, cu, want, poll_ctr(ws, 0, cu), budget);
      else VMWAITN(NX - NE);
      BAR0();
    }
  };
#pragma nounroll
  for (int l = 0; l < 4; l += 2) {
    layer(IC<0>{}, l);
    layer(IC<1>{}, l + 1);
  }
  {
    unsigned char* o = (unsigned char*)p.out + (32 * tt2 + r) * 1280 + h * 16 + nq * 320;
    *(v4u*)(o + w * 64) = v4u{pk2(xr[0], xr[1]), pk2(xr[2], xr[3]), pk2(xr[4], xr[5]), pk2(xr[6], xr[7])};
    *(v4u*)(o + w * 64 + 32) = v4u{pk2(xr[8], xr[9]), pk2(xr[10], xr[11]), pk2(xr[12], xr[13]), pk2(xr[14], xr[15])};
    if (w < 2) *(v4u*)(o + 256 + w * 32) = v4u{pkv(xr4[0]), pkv(xr4[1]), pkv(xr4[2]), pkv(xr4[3])};
    if (blockIdx.x == 0 && tid == 0) __hip_atomic_store((gu64*)(ws + EP_OFF), tag, RLX_AGENT);
  }
}

extern "C" void kernel_launch(void* const* d_in, const int* in_sizes, int n_in, void* d_out, int out_size, void* d_ws, size_t ws_size,
                              hipStream_t stream) {
  Params p;
  p.x0 = (const u16*)d_in[0]; p.wA = (const u16*)d_in[1]; p.woA = (const u16*)d_in[2];
  p.wB = (const u16*)d_in[4]; p.wphi = (const u16*)d_in[5]; p.woB = (const u16*)d_in[6];
  p.out = (u16*)d_out; p.ws = (unsigned char*)d_ws;
  (void)hipFuncSetAttribute((const void*)main_kernel, hipFuncAttributeMaxDynamicSharedMemorySize, L_BYTES);
  main_kernel<<<256, 256, L_BYTES, stream>>>(p);
}
```
